```python
import torch

def _softmax(x, axis=-1):
    return torch.softmax(x, dim=axis)
def _gelu(x):
    return torch.nn.functional.gelu(x, approximate="tanh")
def _silu(x):
    return torch.nn.functional.silu(x)
def _sigmoid(x):
    return torch.sigmoid(x)
def _layernorm(x, axis=-1, eps=1e-5):
    m = x.mean(dim=axis, keepdim=True)
    v = x.var(dim=axis, keepdim=True, unbiased=False)
    return (x - m) / torch.sqrt(v + eps)
def _scatter_add(tgt, idx, src, axis=0):
    return tgt.clone().index_add_(axis, idx, src)
def _segment_sum(src, seg, n):
    out = torch.zeros((n,) + src.shape[1:], dtype=src.dtype)
    return out.index_add_(0, seg, src)
def _pad(x, pw):
    flat = tuple(v for pair in reversed(pw) for v in pair)
    return torch.nn.functional.pad(x, flat)

kernel_name = 'recurrent_scan__ssm_conv_s4d__dense_activation_gelu__prompt'

def setup_inputs():

    _w = (lambda a: (lambda t: t.to(torch.bfloat16) if t.is_floating_point() else t)(torch.as_tensor(a)))
    def _A_setup():
        Bsz, L, D, N = 1, 2048, 512, 128
        return {
            "u": _w((lambda *s: torch.randn(*s, dtype=torch.bfloat16) * 0.05)(Bsz, L, D)),
            "log_A_real": _w((lambda *s: torch.randn(*s, dtype=torch.bfloat16) * 6.4)(D, N) * 0.5),
            "A_imag": _w((lambda *s: torch.randn(*s, dtype=torch.bfloat16) * 0.2)(D, N) * 3.14),
            "C_real": _w((lambda *s: torch.randn(*s, dtype=torch.bfloat16) * 0.4)(D, N) / N ** 0.5),
            "C_imag": _w((lambda *s: torch.randn(*s, dtype=torch.bfloat16) * 0.4)(D, N) / N ** 0.5),
            "log_dt": _w((lambda *s: torch.randn(*s, dtype=torch.bfloat16) * 0.2)(D) * 0.5 - 4.0),
            "D_skip": _w((lambda *s: torch.randn(*s, dtype=torch.bfloat16) * 0.2)(D)),
        }
    _A_in = _A_setup()

    def _B_setup():
        B, S, D, F = 1, 2048, 512, 1792
        return {
            "x": _w((lambda *s: torch.randn(*s, dtype=torch.bfloat16) * 0.05)(B, S, D)),
            "w1": _w((lambda *s: torch.randn(*s, dtype=torch.bfloat16) * 0.8)(D, F) * (D ** -0.5)),
            "b1": _w((lambda *s: torch.randn(*s, dtype=torch.bfloat16) * 0.1)(F) * 0.01),
            "w2": _w((lambda *s: torch.randn(*s, dtype=torch.bfloat16) * 0.1)(F, D) * (F ** -0.5)),
            "b2": _w((lambda *s: torch.randn(*s, dtype=torch.bfloat16) * 0.1)(D) * 0.01),
        }
    _B_in = _B_setup()

    return {
        "A_u": _A_in["u"],
        "A_log_A_real": _A_in["log_A_real"],
        "A_A_imag": _A_in["A_imag"],
        "A_C_real": _A_in["C_real"],
        "A_C_imag": _A_in["C_imag"],
        "A_log_dt": _A_in["log_dt"],
        "A_D_skip": _A_in["D_skip"],
        "B_x": _B_in["x"],
        "B_w1": _B_in["w1"],
        "B_b1": _B_in["b1"],
        "B_w2": _B_in["w2"],
        "B_b2": _B_in["b2"],
    }

def reference(A_u, A_log_A_real, A_A_imag, A_C_real, A_C_imag, A_log_dt, A_D_skip, B_x, B_w1, B_b1, B_w2, B_b2):
    def _A_ref(u, log_A_real, A_imag, C_real, C_imag, log_dt, D_skip):
        L = u.shape[1]
        dt = torch.exp(log_dt)
        A = -torch.exp(log_A_real) + 1j * A_imag
        C = (C_real + 1j * C_imag) * dt[:, None]
        pos = (lambda x, dt: x.to(dt))(torch.arange(L), log_dt.dtype)
        P = torch.exp((A * dt[:, None])[:, :, None] * pos[None, None, :])
        K = 2.0 * torch.einsum("dn,dnl->dl", C, P).real
        L2 = 2 * L
        u_t = (lambda x, *d: x.permute(*d))(u, 0, 2, 1)
        Kf = (lambda x, *a, **k: torch.fft.rfft(x.float(), *a, **k))(K, L2)
        Uf = (lambda x, *a, **k: torch.fft.rfft(x.float(), *a, **k))(u_t, L2)
        y = (lambda x, *a, **k: torch.fft.irfft(x, *a, **k).to(torch.bfloat16))(Uf * Kf[None, :, :], L2)[:, :, :L]
        y = (lambda x, *d: x.permute(*d))(y, 0, 2, 1)
        return y + u * D_skip[None, None, :]

    def _B_ref(x, w1, b1, w2, b2):
        h = torch.einsum("bsd,df->bsf", x, w1) + b1
        h = _gelu(h)
        return torch.einsum("bsf,fd->bsd", h, w2) + b2

    x = A_u
    xn = _layernorm(x, axis=-1)
    s = _A_ref(xn, A_log_A_real, A_A_imag, A_C_real, A_C_imag, A_log_dt, A_D_skip)
    g = _silu(_B_ref(xn, B_w1, B_b1, B_w2, B_b2))
    return x + s * g
```

```cpp
#include <hip/hip_runtime.h>
#include <hip/hip_bf16.h>
#include <stdint.h>
typedef unsigned short u16;
__device__ __forceinline__ float bf2f(u16 v){ return __uint_as_float(((unsigned)v)<<16); }
__device__ __forceinline__ u16 f2bf(float f){ unsigned u=__float_as_uint(f); u += 0x7fffu + ((u>>16)&1u); return (u16)(u>>16); }
__device__ __forceinline__ float rbf(float f){ return bf2f(f2bf(f)); }
constexpr int L=2048, D=512, N=128, F=1792;

__global__ void k_ln(const u16* x, u16* xn){
  int t=blockIdx.x; int lane=threadIdx.x;
  float v[8]; float s=0;
  for(int j=0;j<8;j++){ v[j]=bf2f(x[t*D+lane*8+j]); s+=v[j]; }
  for(int o=32;o;o>>=1) s+=__shfl_xor(s,o);
  float mu=s/D; float q=0;
  for(int j=0;j<8;j++){ float d=v[j]-mu; q+=d*d; }
  for(int o=32;o;o>>=1) q+=__shfl_xor(q,o);
  float m=rbf(mu); float var=rbf(q/D); float sd=rbf(sqrtf(rbf(var+1e-5f)));
  for(int j=0;j<8;j++){ xn[t*D+lane*8+j]=f2bf(rbf(v[j]-m)/sd); }
}
__global__ void k_kgen(const u16* lar,const u16* ai,const u16* cr,const u16* ci,const u16* ldt,float* K){
  int d=blockIdx.x;
  __shared__ float zr[N],zi[N],c_r[N],c_i[N];
  float dt=rbf(expf(bf2f(ldt[d])));
  if(threadIdx.x<N){ int n=threadIdx.x;
    zr[n]=-rbf(expf(bf2f(lar[d*N+n])))*dt; zi[n]=bf2f(ai[d*N+n])*dt; c_r[n]=bf2f(cr[d*N+n])*dt; c_i[n]=bf2f(ci[d*N+n])*dt; }
  __syncthreads();
  for(int l=threadIdx.x;l<L;l+=blockDim.x){
    float p=(float)l;
    float acc=0;
    for(int n=0;n<N;n++){ float e=expf(zr[n]*p); float s,c; sincosf(zi[n]*p,&s,&c); acc+= e*(c_r[n]*c - c_i[n]*s); }
    K[d*L+l]=2.f*acc;
  }
}
__global__ void k_conv(const u16* xn,const float* K,const u16* dskip,u16* s){
  int d=blockIdx.x;
  __shared__ float u[L], k[L];
  for(int l=threadIdx.x;l<L;l+=blockDim.x){ u[l]=bf2f(xn[l*D+d]); k[l]=K[d*L+l]; }
  __syncthreads();
  float ds=bf2f(dskip[d]);
  for(int t=threadIdx.x;t<L;t+=blockDim.x){
    float acc=0; for(int l=0;l<=t;l++) acc+=k[l]*u[t-l];
    s[t*D+d]=f2bf(rbf(acc)+rbf(u[t]*ds));
  }
}
__global__ void k_g1(const u16* xn,const u16* w1,const u16* b1,u16* h){
  int t=blockIdx.y; int f=blockIdx.x*256+threadIdx.x;
  __shared__ float xs[D];
  for(int i=threadIdx.x;i<D;i+=256) xs[i]=bf2f(xn[t*D+i]);
  __syncthreads();
  float acc=0; for(int k=0;k<D;k++) acc+=xs[k]*bf2f(w1[k*F+f]);
  float v=rbf(rbf(acc)+bf2f(b1[f]));
  float g=0.5f*v*(1.f+tanhf(0.7978845608028654f*(v+0.044715f*v*v*v)));
  h[t*F+f]=f2bf(g);
}
__global__ void k_g2(const u16* h,const u16* w2,const u16* b2,const u16* x,const u16* s,u16* out){
  int t=blockIdx.y; int d=blockIdx.x*256+threadIdx.x;
  __shared__ float hs[F];
  for(int i=threadIdx.x;i<F;i+=256) hs[i]=bf2f(h[t*F+i]);
  __syncthreads();
  float acc=0; for(int k=0;k<F;k++) acc+=hs[k]*bf2f(w2[k*D+d]);
  float v=rbf(rbf(acc)+bf2f(b2[d]));
  float g=rbf(v/(1.f+expf(-v)));
  out[t*D+d]=f2bf(bf2f(x[t*D+d])+rbf(bf2f(s[t*D+d])*g));
}
extern "C" void kernel_launch(void* const* d_in, const int* in_sizes, int n_in,
                              void* d_out, int out_size, void* d_ws, size_t ws_size,
                              hipStream_t stream) {
  const u16* x=(const u16*)d_in[0];
  char* ws=(char*)d_ws;
  u16* xn=(u16*)ws; float* K=(float*)(ws+(4<<20)); u16* s=(u16*)(ws+(12<<20)); u16* h=(u16*)(ws+(16<<20));
  k_ln<<<L,64,0,stream>>>(x,xn);
  k_kgen<<<D,256,0,stream>>>((const u16*)d_in[1],(const u16*)d_in[2],(const u16*)d_in[3],(const u16*)d_in[4],(const u16*)d_in[5],K);
  k_conv<<<D,256,0,stream>>>(xn,K,(const u16*)d_in[6],s);
  k_g1<<<dim3(F/256,L),256,0,stream>>>(xn,(const u16*)d_in[8],(const u16*)d_in[9],h);
  k_g2<<<dim3(D/256,L),256,0,stream>>>(h,(const u16*)d_in[10],(const u16*)d_in[11],x,s,(u16*)d_out);
}
```

```cpp
#include <hip/hip_runtime.h>
#include <stdint.h>

typedef unsigned short u16;
typedef __attribute__((ext_vector_type(8))) __bf16 bf16x8;
typedef __attribute__((ext_vector_type(2))) __bf16 bf16x2;
typedef __attribute__((ext_vector_type(8))) _Float16 f16x8;
typedef __attribute__((ext_vector_type(2))) _Float16 f16x2;
typedef __attribute__((ext_vector_type(4))) short s16x4;
typedef __attribute__((ext_vector_type(2))) float f32x2;
typedef __attribute__((ext_vector_type(4))) float f32x4;
typedef __attribute__((ext_vector_type(16))) float f32x16;
typedef __attribute__((ext_vector_type(4))) unsigned u32x4;
typedef __attribute__((ext_vector_type(2))) unsigned u32x2;
#define LAS __attribute__((address_space(3)))
#define GAS __attribute__((address_space(1)))
#define DI __device__ __forceinline__

constexpr int L_ = 2048, D_ = 512, N_ = 128, F_ = 1792;

DI float bf2f(u16 v) { return __uint_as_float(((unsigned)v) << 16); }
DI float bflo(unsigned v) { return __uint_as_float(v << 16); }
DI float bfhi(unsigned v) { return __uint_as_float(v & 0xffff0000u); }
DI unsigned pk_bf16(float a, float b) { f32x2 v = {a, b}; return __builtin_bit_cast(unsigned, __builtin_convertvector(v, bf16x2)); }
DI unsigned pk_f16(float a, float b) { f32x2 v = {a, b}; return __builtin_bit_cast(unsigned, __builtin_convertvector(v, f16x2)); }
DI unsigned pk_f16v(f32x2 v) { return __builtin_bit_cast(unsigned, __builtin_convertvector(v, f16x2)); }
DI bf16x2 as_bf2(unsigned v) { return __builtin_bit_cast(bf16x2, v); }
DI f16x2 as_h2(unsigned v) { return __builtin_bit_cast(f16x2, v); }
DI bf16x8 as_bf8(u32x4 v) { return __builtin_bit_cast(bf16x8, v); }
DI f16x8 as_h8(u32x4 v) { return __builtin_bit_cast(f16x8, v); }
DI float rbf(float a) { return bflo(pk_bf16(a, a)); }
DI unsigned bfbits(float a) { return pk_bf16(a, a) & 0xffffu; }
DI float both_halves(float v) {
  auto r = __builtin_amdgcn_permlane32_swap(__float_as_uint(v), __float_as_uint(v), false, false);
  return __uint_as_float(r[0]) + __uint_as_float(r[1]);
}
DI void lo_hi(float v, float& lo, float& hi) {
  auto r = __builtin_amdgcn_permlane32_swap(__float_as_uint(v), __float_as_uint(v), false, false);
  lo = __uint_as_float(r[0]); hi = __uint_as_float(r[1]);
}
DI float wave_absmax(float x) {
  unsigned v = __float_as_uint(x);
  v = max(v, (unsigned)__builtin_amdgcn_update_dpp(0, v, 0xB1, 0xf, 0xf, true));
  v = max(v, (unsigned)__builtin_amdgcn_update_dpp(0, v, 0x4E, 0xf, 0xf, true));
  v = max(v, (unsigned)__builtin_amdgcn_update_dpp(0, v, 0x141, 0xf, 0xf, true));
  v = max(v, (unsigned)__builtin_amdgcn_update_dpp(0, v, 0x140, 0xf, 0xf, true));
  unsigned a = __builtin_amdgcn_readlane(v, 0), b = __builtin_amdgcn_readlane(v, 16), c = __builtin_amdgcn_readlane(v, 32),
           d = __builtin_amdgcn_readlane(v, 48);
  return __uint_as_float(max(max(a, b), max(c, d)));
}
DI float next_lane(float v) { return __uint_as_float(__builtin_amdgcn_update_dpp(0, __float_as_uint(v), 0x130, 0xf, 0xf, true)); }
DI float rdlane(float v, int l) { return __uint_as_float(__builtin_amdgcn_readlane(__float_as_uint(v), l)); }

#define WAIT_VM(n) asm volatile("s_waitcnt vmcnt(" #n ")" ::: "memory")
#define SCHED() __builtin_amdgcn_sched_barrier(0)
#define WAIT_LGKM0() asm volatile("s_waitcnt lgkmcnt(0)" ::: "memory")
#define BARRIER() do { SCHED(); asm volatile("s_waitcnt lgkmcnt(0)" ::: "memory"); __builtin_amdgcn_s_barrier(); asm volatile("" ::: "memory"); SCHED(); } while (0)

constexpr size_t WS_HF = 0;
constexpr size_t WS_W2F = WS_HF + (size_t)64 * 112 * 1024;
constexpr size_t WS_UT = WS_W2F + (size_t)16 * 112 * 1024;
constexpr size_t WS_ST = WS_UT + (size_t)512 * 4096;
constexpr unsigned WS_END = (unsigned)(WS_ST + (size_t)512 * 4096);

#define ST_DECL
#define ST()
#define ST_OUT(kid)

constexpr int STG = 36864;
constexpr int K1_SCR = 4 * STG;
constexpr int K1_LDS = K1_SCR + 8 * 64 * 8;

DI void dma_x(unsigned lds, const char* xb, unsigned vx) {
  asm volatile("s_mov_b32 m0, %2\n\ts_nop 0\n\tglobal_load_lds_dwordx4 %0, %1" ::"v"(vx), "s"(xb), "s"(lds) : "memory");
}
DI void dma_w(unsigned lds, const char* wb, const unsigned (&vw)[4]) {
  asm volatile(
      "s_mov_b32 m0, %5\n\ts_nop 0\n\t"
      "global_load_lds_dwordx4 %0, %4\n\t"
      "global_load_lds_dwordx4 %1, %4 offset:1024\n\t"
      "global_load_lds_dwordx4 %2, %4 offset:2048\n\t"
      "global_load_lds_dwordx4 %3, %4 offset:3072" ::"v"(vw[0]),
      "v"(vw[1]), "v"(vw[2]), "v"(vw[3]), "s"(wb), "s"(lds)
      : "memory");
}
DI void stat_acc(u32x4 v, float piv, f32x2& s1, f32x2& s2) {
#pragma unroll
  for (int i = 0; i < 4; ++i) {
    f32x2 d = {bflo(v[i]) - piv, bfhi(v[i]) - piv};
    s1 += d;
    s2 = __builtin_elementwise_fma(d, d, s2);
  }
}

__global__ __launch_bounds__(512, 2) void k1_ffn_up(const u16* __restrict__ x, const u16* __restrict__ w1,
                                                   const u16* __restrict__ b1, const u16* __restrict__ w2,
                                                   char* __restrict__ ws) {
  extern __shared__ __attribute__((aligned(16))) char smem[];
  const int tid = threadIdx.x, lane = tid & 63;
  const int w = __builtin_amdgcn_readfirstlane(tid >> 6);
  const int b = blockIdx.x, xcd = b & 7, jj = b >> 3;
  const int rb = (xcd >> 1) * 8 + (jj & 7), cb = (xcd & 1) * 4 + (jj >> 3);
  const int r = lane & 31, h = lane >> 5;
  const unsigned lds0 = (unsigned)(size_t)smem;
  ST_DECL; ST();
  const char* xb = (const char*)x;
  const char* wb = (const char*)w1;
  unsigned vx;
  {
    const int tk = w * 8 + (lane >> 3);
    vx = (unsigned)((rb * 64 + tk) * 1024 + (((lane & 7) ^ ((tk >> 1) & 7)) << 4));
  }
  const unsigned ldx = lds0 + w * 1024;
  const unsigned soff = lane * 128 + ((w ^ ((lane >> 1) & 7)) << 4);
  const unsigned poff = lane * 128 + (((lane >> 1) & 7) << 4);
  f32x2 s1 = {0.f, 0.f}, s2 = {0.f, 0.f};
  float piv;
  u32x4 xk = {0, 0, 0, 0};
  f32x16 acc[2];
  float cs = 0.f;
  u16 b1v = 0;

  if (w < 7) {
    unsigned vw[4];
#pragma unroll
    for (int m = 0; m < 4; ++m) {
      int c = (4 * w + m) * 64 + lane, row = c / 28, cc = c - row * 28;
      vw[m] = (unsigned)(row * 3584 + cb * 448 + cc * 16 - m * 1024);
    }
    const unsigned ldw = lds0 + 8192 + w * 4096;
#pragma unroll
    for (int s = 0; s < 4; ++s) {
      dma_x(ldx + s * STG, xb + s * 128, vx);
      dma_w(ldw + s * STG, wb + (size_t)s * 64 * 3584, vw);
    }
    const int blk = (lane >> 4) & 1, q4 = (lane & 15) >> 2, p4 = lane & 3;
    const unsigned aoff = 8192 + (8 * h + q4) * 448 + (w * 32 + 16 * blk + 4 * p4) * 2;
    unsigned boff[4];
#pragma unroll
    for (int k = 0; k < 4; ++k) boff[k] = r * 128 + (((2 * k + h) ^ ((r >> 1) & 7)) << 4);
#pragma unroll
    for (int i = 0; i < 16; ++i) { acc[0][i] = 0.f; acc[1][i] = 0.f; }
    float c4[4] = {0.f, 0.f, 0.f, 0.f};
    const bf16x2 ones = as_bf2(0x3F803F80u);
    u32x4 A[2][4], B0[2][4], B1[2][4], SX[2];
#pragma unroll
    for (int s = 0; s <= 8; ++s) {
      if (s < 8) {
        if (s == 0) WAIT_VM(15); else if (s <= 5) WAIT_VM(10); else if (s == 6) WAIT_VM(5); else WAIT_VM(0);
        BARRIER();
        if (s == 0 || s == 1 || s == 4 || s == 7) ST();
        if (s >= 1 && s + 3 <= 7) {
          dma_x(ldx + ((s + 3) & 3) * STG, xb + (s + 3) * 128, vx);
          dma_w(ldw + ((s + 3) & 3) * STG, wb + (size_t)(s + 3) * 64 * 3584, vw);
        }
        const char* sb = smem + (s & 3) * STG;
        if (s == 0) piv = bf2f(*(const u16*)(sb + poff));
#pragma unroll
        for (int k = 0; k < 4; ++k) {
          u32x2 al = __builtin_bit_cast(u32x2, __builtin_amdgcn_ds_read_tr16_b64_v4i16((LAS s16x4*)(sb + aoff + (16 * k) * 448)));
          u32x2 ah = __builtin_bit_cast(u32x2, __builtin_amdgcn_ds_read_tr16_b64_v4i16((LAS s16x4*)(sb + aoff + (16 * k + 4) * 448)));
          A[s & 1][k] = (u32x4){al[0], al[1], ah[0], ah[1]};
          B0[s & 1][k] = *(const u32x4*)(sb + boff[k]);
          B1[s & 1][k] = *(const u32x4*)(sb + 4096 + boff[k]);
        }
        SX[s & 1] = *(const u32x4*)(sb + soff);
        SCHED();
      }
      if (s >= 1) {
        const int p = (s - 1) & 1;
#pragma unroll
        for (int k = 0; k < 4; ++k) {
          acc[0] = __builtin_amdgcn_mfma_f32_32x32x16_bf16(as_bf8(A[p][k]), as_bf8(B0[p][k]), acc[0], 0, 0, 0);
          acc[1] = __builtin_amdgcn_mfma_f32_32x32x16_bf16(as_bf8(A[p][k]), as_bf8(B1[p][k]), acc[1], 0, 0, 0);
#pragma unroll
          for (int i = 0; i < 4; ++i) c4[i] = __builtin_amdgcn_fdot2_f32_bf16(as_bf2(A[p][k][i]), ones, c4[i], false);
        }
        stat_acc(SX[p], piv, s1, s2);
        if (s - 1 == cb) xk = SX[p];
#pragma unroll
        for (int k = 0; k < 8; ++k) {
          __builtin_amdgcn_sched_group_barrier(0x8, 1, 0);
          __builtin_amdgcn_sched_group_barrier(0x2, 5, 0);
        }
        SCHED();
      }
      if (s == 7) b1v = b1[cb * 224 + w * 32 + r];
    }
    cs = both_halves((c4[0] + c4[1]) + (c4[2] + c4[3]));
  } else {
#pragma unroll
    for (int s = 0; s < 4; ++s) dma_x(ldx + s * STG, xb + s * 128, vx);
    SCHED();
    unsigned w2v[7][8];
    {
      const auto rw = __builtin_amdgcn_make_buffer_rsrc((void*)w2, 0, F_ * D_ * 2, 0x00020000);
      const unsigned vo = h * 4096 + r * 2;
#pragma unroll
      for (int f = 0; f < 7; ++f) {
        const int gw = b * 7 + f, dt = gw / 112, ks = gw - dt * 112;
        const unsigned so = ks * 16384 + dt * 64;
#pragma unroll
        for (int j = 0; j < 8; ++j)
          w2v[f][j] = __builtin_amdgcn_raw_buffer_load_b16(rw, vo + (j & 3) * 1024, so + (j >> 2) * 8192, 0);
      }
    }
    SCHED();
#pragma unroll
    for (int s = 0; s < 8; ++s) {
      if (s == 0) WAIT_VM(59); else if (s == 1) WAIT_VM(58); else if (s == 2) WAIT_VM(58); else if (s == 3) WAIT_VM(58);
      else if (s <= 5) WAIT_VM(2); else if (s == 6) WAIT_VM(1); else WAIT_VM(0);
      BARRIER();
      if (s == 0 || s == 1 || s == 4 || s == 7) ST();
      if (s >= 1 && s + 3 <= 7) dma_x(ldx + ((s + 3) & 3) * STG, xb + (s + 3) * 128, vx);
      const char* sb = smem + (s & 3) * STG;
      if (s == 0) piv = bf2f(*(const u16*)(sb + poff));
      u32x4 sx = *(const u32x4*)(sb + soff);
      stat_acc(sx, piv, s1, s2);
      if (s == cb) xk = sx;
    }
#pragma unroll
    for (int f = 0; f < 7; ++f) {
      asm volatile("" : "+v"(w2v[f][0]), "+v"(w2v[f][1]), "+v"(w2v[f][2]), "+v"(w2v[f][3]), "+v"(w2v[f][4]), "+v"(w2v[f][5]),
                   "+v"(w2v[f][6]), "+v"(w2v[f][7]));
      u32x4 v = {w2v[f][0] | (w2v[f][1] << 16), w2v[f][2] | (w2v[f][3] << 16), w2v[f][4] | (w2v[f][5] << 16),
                 w2v[f][6] | (w2v[f][7] << 16)};
      *(u32x4*)(ws + WS_W2F + (size_t)(b * 7 + f) * 1024 + lane * 16) = v;
    }
    {
      const u32x4 sent = {~0u, ~0u, ~0u, ~0u};
      char* sp = ws + WS_ST + (size_t)b * 8192 + lane * 16;
#pragma unroll
      for (int i = 0; i < 8; ++i) *(u32x4*)(sp + i * 1024) = sent;
    }
  }
  ST();
  *(f32x2*)(smem + K1_SCR + (w * 64 + lane) * 8) = (f32x2){s1[0] + s1[1], s2[0] + s2[1]};
  BARRIER();
  float m_, sd_, rho_;
  {
    f32x2 a = {0.f, 0.f};
#pragma unroll
    for (int i = 0; i < 8; ++i) a += *(const f32x2*)(smem + K1_SCR + (i * 64 + lane) * 8);
    const float dm = a[0] * (1.f / 512.f);
    const float var = fmaxf(fmaf(-dm, dm, a[1] * (1.f / 512.f)), 0.f);
    m_ = rbf(piv + dm);
    sd_ = rbf(__builtin_amdgcn_sqrtf(rbf(rbf(var) + 1e-5f)));
    rho_ = __builtin_amdgcn_rcpf(sd_);
  }
  ST();
  if (w < 7) {
    float mb[2], sd[2], rho[2];
    lo_hi(m_, mb[0], mb[1]); lo_hi(sd_, sd[0], sd[1]); lo_hi(rho_, rho[0], rho[1]);
    const float hi = rbf(cs), r1 = cs - hi, mid = rbf(r1), lo = r1 - mid;
    u32x4 a = {pk_bf16(hi, mid), bfbits(lo) | ((unsigned)b1v << 16), 0u, 0u};
    if (h) { a[0] = 0u; a[1] = 0u; }
#pragma unroll
    for (int t = 0; t < 2; ++t) {
      u32x4 bb = {pk_bf16(-mb[t], -mb[t]), pk_bf16(-mb[t], sd[t]), 0u, 0u};
      acc[t] = __builtin_amdgcn_mfma_f32_32x32x16_bf16(as_bf8(a), as_bf8(bb), acc[t], 0, 0, 0);
    }
#pragma unroll
    for (int t = 0; t < 2; ++t) {
      const float ca = -2.302208198f * rho[t], cbb = -0.1029432397f * rho[t] * rho[t] * rho[t], sdv = sd[t];
      char* hp = ws + WS_HF + ((size_t)((rb * 2 + t) * 112 + cb * 14 + 2 * w) * 64 + lane) * 16;
#pragma unroll
      for (int hh = 0; hh < 2; ++hh) {
        unsigned o[4];
#pragma unroll
        for (int i = 0; i < 4; ++i) {
          float g[2];
#pragma unroll
          for (int e = 0; e < 2; ++e) {
            float d = acc[t][8 * hh + 2 * i + e];
            float ex = __builtin_amdgcn_exp2f(d * fmaf(d * d, cbb, ca));
            g[e] = d * __builtin_amdgcn_rcpf(fmaf(ex, sdv, sdv));
          }
          o[i] = pk_bf16(g[0], g[1]);
        }
        *(u32x4*)(hp + hh * 1024) = (u32x4){o[0], o[1], o[2], o[3]};
      }
    }
  }
  ST();
  {
    _Float16* ut = (_Float16*)(ws + WS_UT) + (size_t)(cb * 64 + w * 8) * 2048 + (2047 - rb * 64 - lane);
#pragma unroll
    for (int i = 0; i < 4; ++i) {
      unsigned d = pk_bf16(bflo(xk[i]) - m_, bfhi(xk[i]) - m_);
      unsigned n = pk_bf16(bflo(d) * rho_, bfhi(d) * rho_);
      f16x2 hv = as_h2(pk_f16(bflo(n), bfhi(n)));
      ut[(size_t)(2 * i) * 2048] = hv[0];
      ut[(size_t)(2 * i + 1) * 2048] = hv[1];
    }
  }
  ST();
}

constexpr int CP_STRIDE = 4352;
constexpr int CV_CP = 0;
constexpr int CV_TAB = 53 * 272;
constexpr int CV_U = 8 * CP_STRIDE;
constexpr int CV_XK = CV_U + 4608;
constexpr int CV_XY = CV_XK + 6144;
constexpr int CV_BYTES = CV_XY + 8192;
constexpr int K2_FLAG = 2 * CV_BYTES;
constexpr int K2_LDS = K2_FLAG + 64;
#define CP_OFF(sig) (((0xDC885410u >> (4 * (sig))) & 15u) * 16u + (sig) * CP_STRIDE)

DI void lds_set(LAS unsigned* p, unsigned v) {
  WAIT_LGKM0();
  __hip_atomic_store(p, v, __ATOMIC_RELAXED, __HIP_MEMORY_SCOPE_WORKGROUP);
  asm volatile("" ::: "memory");
}
DI void lds_wait(LAS unsigned* p, unsigned v) {
  while ((unsigned)__builtin_amdgcn_readfirstlane(__hip_atomic_load(p, __ATOMIC_RELAXED, __HIP_MEMORY_SCOPE_WORKGROUP)) < v)
    __builtin_amdgcn_s_sleep(0);
  asm volatile("" ::: "memory");
}
DI f32x2 cmul(f32x2 a, f32x2 b, f32x2 bn) {
  f32x2 r2 = {a[0], a[0]}, i2 = {a[1], a[1]};
  return __builtin_elementwise_fma(i2, bn, r2 * b);
}
DI f32x2 rot(f32x2 b) { return (f32x2){-b[1], b[0]}; }
DI f32x2 cexp_p(float zr2, float zi, float p) {
  float th = zi * p;
  float n = __builtin_rintf(th * 0.15915494f);
  float rr = fmaf(th, 0.15915494f, -n);
  rr = fmaf(th, 6.4206382e-9f, rr);
  float mag = __builtin_amdgcn_exp2f(zr2 * p);
  return (f32x2){mag * __builtin_amdgcn_cosf(rr), mag * __builtin_amdgcn_sinf(rr)};
}

__global__ __launch_bounds__(512, 2) void k2_down_conv(const u16* __restrict__ x, const u16* __restrict__ lar,
                                                      const u16* __restrict__ aim, const u16* __restrict__ cre,
                                                      const u16* __restrict__ cim, const u16* __restrict__ ldt,
                                                      const u16* __restrict__ dskip, const u16* __restrict__ b2,
                                                      char* __restrict__ ws, u16* __restrict__ out) {
  extern __shared__ __attribute__((aligned(16))) char smem[];
  const int tid = threadIdx.x, lane = tid & 63;
  const int w = __builtin_amdgcn_readfirstlane(tid >> 6);
  const int b = blockIdx.x, xcd = b & 7, jj = b >> 3;
  const int rb = (xcd >> 1) * 8 + (jj & 7), cb = (xcd & 1) * 4 + (jj >> 3);
  const int r = lane & 31, h = lane >> 5;
  LAS unsigned* flags = (LAS unsigned*)(smem + K2_FLAG);
  if (tid < 16) flags[tid] = 0u;
  ST_DECL; ST();
  const int cw = w & 3, chl = cw >> 1, hf = cw & 1;
  const int c = 2 * b + chl, n = hf * 64 + lane;
  char* cv = smem + chl * CV_BYTES;
  char* tab = cv + hf * CV_TAB + lane * 4;
  const u16 p_ldt = ldt[c], p_lar = lar[c * N_ + n], p_aim = aim[c * N_ + n];
  const auto rs = __builtin_amdgcn_make_buffer_rsrc((void*)ws, 0, WS_END, 0x00020000);
  const unsigned s_off = (unsigned)WS_ST + (cb * 64 + lane) * 4096 + (rb * 64 + w * 8) * 2;

  u16 xv[8];
  u16 b2v;
  u32x4 sv;
#define PUT(row, v) *(unsigned*)(tab + (row) * 272) = pk_f16v(v)

  if (w < 4) {
    SCHED();
    BARRIER();
    const char* ha = ws + WS_HF + ((size_t)((rb * 2) * 112 + 28 * w) * 64 + lane) * 16;
    const char* wbp = ws + WS_W2F + ((size_t)((cb * 2) * 112 + 28 * w) * 64 + lane) * 16;
    constexpr int P = 8;
    u32x4 fa[P][2], fb[P][2];
#pragma unroll
    for (int k = 0; k < P; ++k) {
      fa[k][0] = *(const u32x4*)(ha + k * 1024);
      fb[k][0] = *(const u32x4*)(wbp + k * 1024);
      fa[k][1] = *(const u32x4*)(ha + 112 * 1024 + k * 1024);
      fb[k][1] = *(const u32x4*)(wbp + 112 * 1024 + k * 1024);
      SCHED();
    }
    {
      const float dt = rbf(expf(bf2f(p_ldt)));
      const float zr2 = -rbf(expf(bf2f(p_lar))) * dt * 1.44269504f, zi = bf2f(p_aim) * dt;
      const f32x2 w8 = cexp_p(zr2, zi, 8.f), w8n = rot(w8);
      f32x2 E = cexp_p(zr2, zi, 64.f), E2 = cexp_p(zr2, zi, 160.f);
      PUT(21 + 8, E); PUT(21 + 20, E2);
#pragma unroll
      for (int k = 1; k < 12; ++k) {
        E = cmul(E, w8, w8n); E2 = cmul(E2, w8, w8n);
        PUT(21 + 8 + k, E); PUT(21 + 20 + k, E2);
      }
      lds_set(flags + 12 + cw, 1u);
    }
    SCHED();
    ST();
    f32x16 acc[2][2];
#pragma unroll
    for (int i = 0; i < 16; ++i) { acc[0][0][i] = 0.f; acc[0][1][i] = 0.f; acc[1][0][i] = 0.f; acc[1][1][i] = 0.f; }
#pragma unroll
    for (int k = 0; k < 28; ++k) {
      const int sl = k % P;
      u32x4 a0 = fa[sl][0], a1 = fa[sl][1], b0 = fb[sl][0], b1 = fb[sl][1];
      acc[0][0] = __builtin_amdgcn_mfma_f32_32x32x16_bf16(as_bf8(a0), as_bf8(b0), acc[0][0], 0, 0, 0);
      acc[0][1] = __builtin_amdgcn_mfma_f32_32x32x16_bf16(as_bf8(a0), as_bf8(b1), acc[0][1], 0, 0, 0);
      acc[1][0] = __builtin_amdgcn_mfma_f32_32x32x16_bf16(as_bf8(a1), as_bf8(b0), acc[1][0], 0, 0, 0);
      acc[1][1] = __builtin_amdgcn_mfma_f32_32x32x16_bf16(as_bf8(a1), as_bf8(b1), acc[1][1], 0, 0, 0);
      SCHED();
      if (k == 0 || k == 8 || k == 16) ST();
      if (k + P < 28) {
        fa[sl][0] = *(const u32x4*)(ha + (k + P) * 1024);
        fb[sl][0] = *(const u32x4*)(wbp + (k + P) * 1024);
        fa[sl][1] = *(const u32x4*)(ha + 112 * 1024 + (k + P) * 1024);
        fb[sl][1] = *(const u32x4*)(wbp + 112 * 1024 + (k + P) * 1024);
      }
      if (k + P == 28) {
#pragma unroll
        for (int j = 0; j < 8; ++j) xv[j] = x[(size_t)(rb * 64 + w * 8 + j) * D_ + cb * 64 + lane];
        b2v = b2[cb * 64 + lane];
      }
      if (k == 25) sv = __builtin_amdgcn_raw_buffer_load_b128(rs, s_off, 0, 16);
      SCHED();
    }
    ST();
    lds_wait(flags + 8, 4u);
#pragma unroll
    for (int a = 0; a < 2; ++a)
#pragma unroll
      for (int bb = 0; bb < 2; ++bb)
#pragma unroll
        for (int g = 0; g < 4; ++g) {
          const int d = 32 * bb + r, ch = 8 * a + 2 * g + h;
          f32x4 v = {acc[a][bb][4 * g], acc[a][bb][4 * g + 1], acc[a][bb][4 * g + 2], acc[a][bb][4 * g + 3]};
          *(f32x4*)(smem + w * 16384 + d * 256 + ((ch ^ (d & 15)) << 4)) = v;
        }
  } else {
    const u16 p_cre = cre[c * N_ + n], p_cim = cim[c * N_ + n], p_ds = dskip[c];
    const char* ug = ws + WS_UT + (size_t)c * 4096 + hf * 2048 + lane * 16;
    const u32x4 u0 = *(const u32x4*)ug, u1 = *(const u32x4*)(ug + 1024);
    SCHED();
    BARRIER();

    const float dt = rbf(expf(bf2f(p_ldt)));
    const float zr2 = -rbf(expf(bf2f(p_lar))) * dt * 1.44269504f, zi = bf2f(p_aim) * dt;
    f32x2 cc = {bf2f(p_cre) * dt, -bf2f(p_cim) * dt};
    const int ce = __builtin_amdgcn_frexp_expf(wave_absmax(fmaxf(fabsf(cc[0]), fabsf(cc[1]))));
    cc *= __builtin_amdgcn_ldexpf(1.f, -ce);
    const float unsc = __builtin_amdgcn_ldexpf(2.f, ce);
    {
      const f32x2 e1 = cexp_p(zr2, -zi, 1.f), w8 = cexp_p(zr2, zi, 8.f), G = cexp_p(zr2, -zi, 256.f);
      const f32x2 e1n = rot(e1), w8n = rot(w8), Gn = rot(G);
      const f32x2 e2 = cmul(e1, e1, e1n), e2n = rot(e2), e4 = cmul(e2, e2, e2n), e4n = rot(e4);
      f32x2 R = cc, E = w8, Gq = cmul(cc, G, Gn), T = Gq;
      PUT(0, R); *(unsigned*)(tab + 21 * 272) = 0x00003C00u; PUT(22, E); PUT(8, Gq);
#pragma unroll
      for (int k = 1; k < 8; ++k) {
        R = cmul(R, e1, e1n); PUT(k, R);
        Gq = cmul(Gq, G, Gn);
        if (k == 1) { PUT(12, Gq); f32x2 q = cmul(Gq, e4, e4n); PUT(13, q); }
        else if (k == 2) { PUT(14, Gq); f32x2 q = cmul(Gq, e4, e4n); PUT(15, q); }
        else PUT(13 + k, Gq);
        if (k < 4) { T = cmul(T, e2, e2n); PUT(8 + k, T); }
        if (k < 7) { E = cmul(E, w8, w8n); PUT(22 + k, E); }
      }
    }
    ST();
    *(u32x4*)(cv + CV_U + hf * 2048 + lane * 16) = u0;
    *(u32x4*)(cv + CV_U + hf * 2048 + 1024 + lane * 16) = u1;
    if (lane < 16) *(u32x4*)(cv + CV_U + 4096 + hf * 256 + lane * 16) = (u32x4){0u, 0u, 0u, 0u};
#pragma unroll
    for (int j = 0; j < 8; ++j) xv[j] = x[(size_t)(rb * 64 + w * 8 + j) * D_ + cb * 64 + lane];
    b2v = b2[cb * 64 + lane];
    lds_wait(flags + 12 + cw, 1u);
    float kx[12];
    {
      f32x16 ka;
#pragma unroll
      for (int i = 0; i < 16; ++i) ka[i] = 0.f;
      const char* tb = cv + hf * CV_TAB + r * 272 + h * 16;
      u32x4 ta[8], tc[8];
#pragma unroll
      for (int k = 0; k < 8; ++k) { ta[k] = *(const u32x4*)(tb + k * 32); tc[k] = *(const u32x4*)(tb + 21 * 272 + k * 32); }
#pragma unroll
      for (int k = 0; k < 8; ++k) ka = __builtin_amdgcn_mfma_f32_32x32x16_f16(as_h8(ta[k]), as_h8(tc[k]), ka, 0, 0, 0);
#pragma unroll
      for (int i = 0; i < 12; ++i) kx[i] = ka[i] * unsc;
      char* xk = cv + CV_XK + hf * 3072 + lane * 16;
#pragma unroll
      for (int g = 0; g < 3; ++g) *(f32x4*)(xk + g * 1024) = (f32x4){kx[4 * g], kx[4 * g + 1], kx[4 * g + 2], kx[4 * g + 3]};
      lds_set(flags + cw, 1u);
      lds_wait(flags + (cw ^ 1), 1u);
      const char* xo = cv + CV_XK + (hf ^ 1) * 3072 + lane * 16;
#pragma unroll
      for (int g = 0; g < 3; ++g) {
        f32x4 o = *(const f32x4*)(xo + g * 1024);
#pragma unroll
        for (int e = 0; e < 4; ++e) kx[4 * g + e] += o[e];
      }
    }
    ST();
    float km = 0.f, km2 = 0.f;
#pragma unroll
    for (int i = 0; i < 8; ++i) km = fmaxf(km, fabsf(kx[i]));
#pragma unroll
    for (int i = 9; i < 12; ++i) km2 = fmaxf(km2, fabsf(kx[i]));
    km = fmaxf(km, h ? 0.f : km2);
    km = fmaxf(km, lane <= 32 ? fabsf(kx[8]) : 0.f);
    const int ke = __builtin_amdgcn_frexp_expf(wave_absmax(km));
    const float ksc = __builtin_amdgcn_ldexpf(1.f, -ke), kun = __builtin_amdgcn_ldexpf(1.f, ke);
#pragma unroll
    for (int i = 0; i < 12; ++i) kx[i] *= ksc;
    {
      char* c0 = cv + CV_CP + 32;
      const bool last = (r == 31);
      const float f512 = rdlane(kx[4], 32), f768 = rdlane(kx[6], 32), f1024 = rdlane(kx[8], 0), f1280 = rdlane(kx[9], 0),
                  f1536 = rdlane(kx[10], 0), f1792 = rdlane(kx[11], 0), f2048 = rdlane(kx[8], 32);
      const float n4 = next_lane(kx[4]), n6 = next_lane(kx[6]);
      *(u32x2*)(c0 + (8 * r + 4 * h) * 2) = (u32x2){pk_f16(kx[0], kx[1]), pk_f16(kx[2], kx[3])};
      {
        const float nn = last ? (h ? f768 : f512) : (h ? n4 : n4);
        const float e1_ = h ? kx[4] : kx[5], e2_ = h ? kx[5] : kx[6], e3_ = h ? nn : kx[7];
        *(u32x4*)(c0 + (256 + 256 * h + 8 * r) * 2) =
            (u32x4){pk_f16(kx[4], kx[4]), pk_f16(e1_, e2_), pk_f16(e2_, e2_), pk_f16(e3_, nn)};
      }
      const float n8 = next_lane(kx[8]), n9 = next_lane(kx[9]), n10 = next_lane(kx[10]), n11 = next_lane(kx[11]);
      {
        const float k0 = h ? kx[6] : kx[8];
        const float nn = last ? (h ? f1024 : f1280) : (h ? n6 : n8);
        const float e1_ = h ? kx[7] : k0, e2_ = h ? kx[7] : ((r & 1) ? nn : k0), e3_ = h ? kx[7] : nn;
        *(u32x4*)(c0 + (h ? 768 + 8 * r : 1024 + 8 * r) * 2) = (u32x4){pk_f16(k0, k0), pk_f16(k0, e1_), pk_f16(e2_, e3_), pk_f16(nn, nn)};
      }
      if (h == 0) {
        const float fl[3] = {f1536, f1792, f2048}, nx[3] = {n9, n10, n11};
#pragma unroll
        for (int a = 0; a < 3; ++a) {
          const float k0 = kx[9 + a], nn = last ? fl[a] : nx[a];
          const unsigned kk = pk_f16(k0, k0), n2 = pk_f16(nn, nn);
          *(u32x4*)(c0 + (1280 + 256 * a + 8 * r) * 2) = (u32x4){kk, kk, (r & 1) ? n2 : pk_f16(k0, nn), n2};
        }
      } else if (r < 2) *(u32x4*)(cv + CV_CP + r * 16) = (u32x4){0u, 0u, 0u, 0u};
    }
    WAIT_LGKM0();
    {
      u32x4 lo[3], hi[3];
      int chs[3];
#pragma unroll
      for (int rd = 0; rd < 3; ++rd) {
        const int idx = min(rd * 64 + lane, 143);
        const int k1 = idx / 18;
        chs[rd] = 16 * hf + (idx - 18 * k1) + 32 * k1;
        lo[rd] = *(const u32x4*)(cv + CV_CP + chs[rd] * 16);
        hi[rd] = *(const u32x4*)(cv + CV_CP + chs[rd] * 16 + 16);
      }
#pragma unroll
      for (int rd = 0; rd < 3; ++rd) {
        const unsigned d[8] = {lo[rd][0], lo[rd][1], lo[rd][2], lo[rd][3], hi[rd][0], hi[rd][1], hi[rd][2], hi[rd][3]};
        unsigned al[7];
#pragma unroll
        for (int i = 0; i < 7; ++i) al[i] = __builtin_amdgcn_alignbit(d[i + 1], d[i], 16);
        char* dst = cv + CV_CP + chs[rd] * 16;
        if (rd < 2 || lane < 16) {
#pragma unroll
          for (int m = 0; m < 4; ++m) {
            if (m) *(u32x4*)(dst + CP_OFF(2 * m)) = (u32x4){d[m], d[m + 1], d[m + 2], d[m + 3]};
            *(u32x4*)(dst + CP_OFF(2 * m + 1)) = (u32x4){al[m], al[m + 1], al[m + 2], al[m + 3]};
          }
        }
      }
    }
    WAIT_LGKM0();
    ST();
    const int i16 = lane & 15, g4 = lane >> 4, a1 = i16 + 1;
    const char* ap = cv + CV_CP + CP_OFF(a1 & 7) + (16 * hf + g4 + (a1 >> 3)) * 16;
    const char* bp = cv + CV_U + (2032 + 128 * hf - 16 * i16 + 8 * g4) * 2;
    f32x4 ya[8];
#pragma unroll
    for (int t = 0; t < 8; ++t) ya[t] = (f32x4){0.f, 0.f, 0.f, 0.f};
    {
      u32x4 A[2][8], B[2][8];
#pragma unroll
      for (int k0 = 0; k0 <= 4; ++k0) {
        if (k0 < 4) {
#pragma unroll
          for (int i = 0; i < 8; ++i) {
            A[k0 & 1][i] = *(const u32x4*)(ap + k0 * 64 + i * 512);
            B[k0 & 1][i] = *(const u32x4*)(bp + k0 * 64 - i * 512);
          }
          SCHED();
        }
        if (k0 >= 1) {
          const int p = (k0 - 1) & 1;
#pragma unroll
          for (int dl = 0; dl < 8; ++dl)
#pragma unroll
            for (int k1 = 0; k1 + dl < 8; ++k1)
              ya[k1 + dl] = __builtin_amdgcn_mfma_f32_16x16x32_f16(as_h8(A[p][k1]), as_h8(B[p][dl]), ya[k1 + dl], 0, 0, 0);
          SCHED();
        }
      }
    }
    ST();
    {
      char* xy = cv + CV_XY + hf * 4096 + lane * 16;
#pragma unroll
      for (int t = 0; t < 8; ++t) asm volatile("" : "+v"(ya[t]));
      f32x4 mine[4];
      if (hf) {
#pragma unroll
        for (int t = 0; t < 4; ++t) { *(f32x4*)(xy + t * 1024) = ya[t]; mine[t] = ya[4 + t]; }
      } else {
#pragma unroll
        for (int t = 0; t < 4; ++t) { *(f32x4*)(xy + t * 1024) = ya[4 + t]; mine[t] = ya[t]; }
      }
      lds_set(flags + 4 + cw, 1u);
      const float dsk = bf2f(p_ds);
      const int tb0 = 1024 * hf + 16 * i16 + 4 * g4;
      float xd[4][4];
#pragma unroll
      for (int t = 0; t < 4; ++t) {
        const u32x2 uu = *(const u32x2*)(cv + CV_U + (2044 - tb0 - 256 * t) * 2);
        const f16x2 ua = as_h2(uu[0]), ub = as_h2(uu[1]);
        const unsigned p0 = pk_bf16((float)ub[1] * dsk, (float)ub[0] * dsk), p1 = pk_bf16((float)ua[1] * dsk, (float)ua[0] * dsk);
        xd[t][0] = bflo(p0); xd[t][1] = bfhi(p0); xd[t][2] = bflo(p1); xd[t][3] = bfhi(p1);
      }
      lds_wait(flags + 4 + (cw ^ 1), 1u);
      const char* xo = cv + CV_XY + (hf ^ 1) * 4096 + lane * 16;
#pragma unroll
      for (int t = 0; t < 4; ++t) {
        const f32x4 y = (*(const f32x4*)(xo + t * 1024) + mine[t]) * kun;
        const unsigned y0 = pk_bf16(y[0], y[1]), y1 = pk_bf16(y[2], y[3]);
        unsigned s0 = pk_bf16(bflo(y0) + xd[t][0], bfhi(y0) + xd[t][1]);
        const unsigned s1 = pk_bf16(bflo(y1) + xd[t][2], bfhi(y1) + xd[t][3]);
        if ((s0 & 0xffffu) == 0xffffu) s0 ^= 0x3fu;
        __hip_atomic_store((GAS unsigned long long*)(ws + WS_ST + (size_t)c * 4096 + (tb0 + 256 * t) * 2),
                           (unsigned long long)s0 | ((unsigned long long)s1 << 32), __ATOMIC_RELAXED, __HIP_MEMORY_SCOPE_AGENT);
      }
    }
    WAIT_LGKM0();
    if (lane == 0) __hip_atomic_fetch_add(flags + 8, 1u, __ATOMIC_RELAXED, __HIP_MEMORY_SCOPE_WORKGROUP);
    ST();
    sv = __builtin_amdgcn_raw_buffer_load_b128(rs, s_off, 0, 16);
  }
  for (unsigned spins = 0; spins < (1u << 20); ++spins) {
    const bool bad = ((sv[0] & 0xffffu) == 0xffffu) || ((sv[2] & 0xffffu) == 0xffffu);
    if (!__any(bad)) break;
    __builtin_amdgcn_s_sleep(4);
    sv = __builtin_amdgcn_raw_buffer_load_b128(rs, s_off, 0, 16);
  }
  ST();
  BARRIER();
  ST();
  {
    f32x4 v0 = {0.f, 0.f, 0.f, 0.f}, v1 = {0.f, 0.f, 0.f, 0.f};
#pragma unroll
    for (int q = 0; q < 4; ++q) {
      v0 += *(const f32x4*)(smem + q * 16384 + lane * 256 + (((2 * w) ^ (lane & 15)) << 4));
      v1 += *(const f32x4*)(smem + q * 16384 + lane * 256 + (((2 * w + 1) ^ (lane & 15)) << 4));
    }
    const float bb = bf2f(b2v);
    u16* op = out + (size_t)(rb * 64 + w * 8) * D_ + cb * 64 + lane;
#pragma unroll
    for (int j = 0; j < 4; ++j) {
      const float a0 = j < 2 ? v0[2 * j] : v1[2 * j - 4], a1 = j < 2 ? v0[2 * j + 1] : v1[2 * j - 3];
      const unsigned pa = pk_bf16(a0, a1);
      const unsigned pv = pk_bf16(bflo(pa) + bb, bfhi(pa) + bb);
      const float q0 = bflo(pv), q1 = bfhi(pv);
      const unsigned pg = pk_bf16(q0 * __builtin_amdgcn_rcpf(1.f + __builtin_amdgcn_exp2f(q0 * -1.44269504f)),
                                  q1 * __builtin_amdgcn_rcpf(1.f + __builtin_amdgcn_exp2f(q1 * -1.44269504f)));
      const unsigned ps = pk_bf16(bflo(sv[j]) * bflo(pg), bfhi(sv[j]) * bfhi(pg));
      const unsigned po = pk_bf16(bf2f(xv[2 * j]) + bflo(ps), bf2f(xv[2 * j + 1]) + bfhi(ps));
      op[(size_t)(2 * j) * D_] = (u16)po;
      op[(size_t)(2 * j + 1) * D_] = (u16)(po >> 16);
    }
  }
}

extern "C" void kernel_launch(void* const* d_in, const int* in_sizes, int n_in, void* d_out, int out_size, void* d_ws,
                              size_t ws_size, hipStream_t stream) {
  (void)in_sizes; (void)n_in; (void)out_size; (void)ws_size;
  (void)hipFuncSetAttribute((const void*)k1_ffn_up, hipFuncAttributeMaxDynamicSharedMemorySize, K1_LDS);
  (void)hipFuncSetAttribute((const void*)k2_down_conv, hipFuncAttributeMaxDynamicSharedMemorySize, K2_LDS);
  const u16* x = (const u16*)d_in[0];
  k1_ffn_up<<<256, 512, K1_LDS, stream>>>(x, (const u16*)d_in[8], (const u16*)d_in[9], (const u16*)d_in[10], (char*)d_ws);
  k2_down_conv<<<256, 512, K2_LDS, stream>>>(x, (const u16*)d_in[1], (const u16*)d_in[2], (const u16*)d_in[3],
                                             (const u16*)d_in[4], (const u16*)d_in[5], (const u16*)d_in[6],
                                             (const u16*)d_in[11], (char*)d_ws, (u16*)d_out);
}
```

```cpp
#include <hip/hip_runtime.h>
#include <stdint.h>

typedef unsigned short u16;
typedef __attribute__((ext_vector_type(8))) __bf16 bf16x8;
typedef __attribute__((ext_vector_type(2))) __bf16 bf16x2;
typedef __attribute__((ext_vector_type(8))) _Float16 f16x8;
typedef __attribute__((ext_vector_type(2))) _Float16 f16x2;
typedef __attribute__((ext_vector_type(4))) short s16x4;
typedef __attribute__((ext_vector_type(2))) float f32x2;
typedef __attribute__((ext_vector_type(4))) float f32x4;
typedef __attribute__((ext_vector_type(16))) float f32x16;
typedef __attribute__((ext_vector_type(4))) unsigned u32x4;
typedef __attribute__((ext_vector_type(2))) unsigned u32x2;
typedef __attribute__((ext_vector_type(8))) unsigned u32x8;
typedef __attribute__((ext_vector_type(16))) __bf16 bf16x16;
#define LAS __attribute__((address_space(3)))
#define GAS __attribute__((address_space(1)))
#define DI __device__ __forceinline__

constexpr int L_ = 2048, D_ = 512, N_ = 128, F_ = 1792;

DI float bf2f(u16 v) { return __uint_as_float(((unsigned)v) << 16); }
DI float bflo(unsigned v) { return __uint_as_float(v << 16); }
DI float bfhi(unsigned v) { return __uint_as_float(v & 0xffff0000u); }
DI unsigned pk_bf16(float a, float b) { f32x2 v = {a, b}; return __builtin_bit_cast(unsigned, __builtin_convertvector(v, bf16x2)); }
DI unsigned pk_f16(float a, float b) { f32x2 v = {a, b}; return __builtin_bit_cast(unsigned, __builtin_convertvector(v, f16x2)); }
DI unsigned pk_f16v(f32x2 v) { return __builtin_bit_cast(unsigned, __builtin_convertvector(v, f16x2)); }
DI bf16x2 as_bf2(unsigned v) { return __builtin_bit_cast(bf16x2, v); }
DI f16x2 as_h2(unsigned v) { return __builtin_bit_cast(f16x2, v); }
DI bf16x8 as_bf8(u32x4 v) { return __builtin_bit_cast(bf16x8, v); }
DI f16x8 as_h8(u32x4 v) { return __builtin_bit_cast(f16x8, v); }
DI float rbf(float a) { return bflo(pk_bf16(a, a)); }
DI unsigned bfbits(float a) { return pk_bf16(a, a) & 0xffffu; }
DI float both_halves(float v) {
  auto r = __builtin_amdgcn_permlane32_swap(__float_as_uint(v), __float_as_uint(v), false, false);
  return __uint_as_float(r[0]) + __uint_as_float(r[1]);
}
DI void lo_hi(float v, float& lo, float& hi) {
  auto r = __builtin_amdgcn_permlane32_swap(__float_as_uint(v), __float_as_uint(v), false, false);
  lo = __uint_as_float(r[0]); hi = __uint_as_float(r[1]);
}
DI float wave_absmax(float x) {
  unsigned v = __float_as_uint(x);
  v = max(v, (unsigned)__builtin_amdgcn_update_dpp(0, v, 0xB1, 0xf, 0xf, true));
  v = max(v, (unsigned)__builtin_amdgcn_update_dpp(0, v, 0x4E, 0xf, 0xf, true));
  v = max(v, (unsigned)__builtin_amdgcn_update_dpp(0, v, 0x141, 0xf, 0xf, true));
  v = max(v, (unsigned)__builtin_amdgcn_update_dpp(0, v, 0x140, 0xf, 0xf, true));
  unsigned a = __builtin_amdgcn_readlane(v, 0), b = __builtin_amdgcn_readlane(v, 16), c = __builtin_amdgcn_readlane(v, 32),
           d = __builtin_amdgcn_readlane(v, 48);
  return __uint_as_float(max(max(a, b), max(c, d)));
}
DI float wave_sum(float x) {
  x += __uint_as_float(__builtin_amdgcn_update_dpp(0, __float_as_uint(x), 0xB1, 0xf, 0xf, true));
  x += __uint_as_float(__builtin_amdgcn_update_dpp(0, __float_as_uint(x), 0x4E, 0xf, 0xf, true));
  x += __uint_as_float(__builtin_amdgcn_update_dpp(0, __float_as_uint(x), 0x141, 0xf, 0xf, true));
  x += __uint_as_float(__builtin_amdgcn_update_dpp(0, __float_as_uint(x), 0x140, 0xf, 0xf, true));
  const unsigned v = __float_as_uint(x);
  return (__uint_as_float(__builtin_amdgcn_readlane(v, 0)) + __uint_as_float(__builtin_amdgcn_readlane(v, 16))) +
         (__uint_as_float(__builtin_amdgcn_readlane(v, 32)) + __uint_as_float(__builtin_amdgcn_readlane(v, 48)));
}
DI float next_lane(float v) { return __uint_as_float(__builtin_amdgcn_update_dpp(0, __float_as_uint(v), 0x130, 0xf, 0xf, true)); }
DI float rdlane(float v, int l) { return __uint_as_float(__builtin_amdgcn_readlane(__float_as_uint(v), l)); }

#define WAIT_VM(n) asm volatile("s_waitcnt vmcnt(" #n ")" ::: "memory")
#define SCHED() __builtin_amdgcn_sched_barrier(0)
#define WAIT_LGKM0() asm volatile("s_waitcnt lgkmcnt(0)" ::: "memory")
#define BARRIER() do { SCHED(); asm volatile("s_waitcnt lgkmcnt(0)" ::: "memory"); __builtin_amdgcn_s_barrier(); asm volatile("" ::: "memory"); SCHED(); } while (0)

constexpr size_t WS_HF = 0;
constexpr size_t WS_W2F = WS_HF + (size_t)64 * 112 * 1024;
constexpr size_t WS_UT = WS_W2F + (size_t)16 * 112 * 1024;
constexpr size_t WS_ST = WS_UT + (size_t)512 * 4096;
constexpr size_t WS_GR = WS_ST + (size_t)512 * 4096;
constexpr size_t WS_EP = WS_GR + (size_t)2048 * 16;
constexpr unsigned WS_END = (unsigned)(WS_EP + 32 * 128);

#define ST_DECL
#define ST()
#define ST_OUT(kid)

constexpr int STG = 36864;
constexpr int K1_LN = 4 * STG;
constexpr int K1_CS = K1_LN + 8192;
constexpr int K1_MS = K1_CS + 1024;
constexpr int K1_LDS = K1_MS + 256;

DI void dma_x(unsigned lds, const char* xb, unsigned vx) {
  asm volatile("s_mov_b32 m0, %2\n\ts_nop 0\n\tglobal_load_lds_dwordx4 %0, %1" ::"v"(vx), "s"(xb), "s"(lds) : "memory");
}
DI void dma_w(unsigned lds, const char* wb, const unsigned (&vw)[4]) {
  asm volatile(
      "s_mov_b32 m0, %5\n\ts_nop 0\n\t"
      "global_load_lds_dwordx4 %0, %4\n\t"
      "global_load_lds_dwordx4 %1, %4 offset:1024\n\t"
      "global_load_lds_dwordx4 %2, %4 offset:2048\n\t"
      "global_load_lds_dwordx4 %3, %4 offset:3072" ::"v"(vw[0]),
      "v"(vw[1]), "v"(vw[2]), "v"(vw[3]), "s"(wb), "s"(lds)
      : "memory");
}
DI void dma_w2(unsigned lds, const char* wb, unsigned v0, unsigned v1) {
  asm volatile(
      "s_mov_b32 m0, %3\n\ts_nop 0\n\t"
      "global_load_lds_dwordx4 %0, %2\n\t"
      "global_load_lds_dwordx4 %1, %2 offset:1024" ::"v"(v0), "v"(v1), "s"(wb), "s"(lds)
      : "memory");
}
DI u32x2 tr_rd3(const LAS char* p) { return __builtin_bit_cast(u32x2, __builtin_amdgcn_ds_read_tr16_b64_v4i16((LAS s16x4*)p)); }
DI u32x2 tr_rd(const char* p) { return __builtin_bit_cast(u32x2, __builtin_amdgcn_ds_read_tr16_b64_v4i16((LAS s16x4*)p)); }

DI void ln_row(u32x4 xr, u32x2 ep, char* smem, char* ws, int w, int lane, int token) {
  float v[8];
#pragma unroll
  for (int i = 0; i < 4; ++i) { v[2 * i] = bflo(xr[i]); v[2 * i + 1] = bfhi(xr[i]); }
  const float mu = wave_sum(((v[0] + v[1]) + (v[2] + v[3])) + ((v[4] + v[5]) + (v[6] + v[7]))) * (1.f / 512.f);
  float q = 0.f;
#pragma unroll
  for (int i = 0; i < 8; ++i) { const float d = v[i] - mu; q = fmaf(d, d, q); }
  const float var = wave_sum(q) * (1.f / 512.f);
  const unsigned mb = bfbits(mu);
  const unsigned sb = bfbits(__builtin_amdgcn_sqrtf(rbf(rbf(var) + 1e-5f)));
  const float m_ = bflo(mb), rho = __builtin_amdgcn_rcpf(bflo(sb));
  const unsigned long long tag = ((((unsigned long long)ep[1] << 32) | ep[0]) + 1ull) << 16;
  const u32x4 gr = {(unsigned)tag | mb, (unsigned)(tag >> 32), (unsigned)tag | sb, (unsigned)(tag >> 32)};
  if (lane == 0) {
    char* gp = ws + WS_GR + (size_t)token * 16;
    asm volatile("global_store_dwordx4 %0, %1, off sc0 sc1\n\ts_nop 1" ::"v"(gp), "v"(gr) : "memory");
  }
  u32x4 o;
#pragma unroll
  for (int i = 0; i < 4; ++i) {
    const unsigned d = pk_bf16(v[2 * i] - m_, v[2 * i + 1] - m_);
    const unsigned n = pk_bf16(bflo(d) * rho, bfhi(d) * rho);
    o[i] = pk_f16(bflo(n), bfhi(n));
  }
  *(u32x4*)(smem + K1_LN + (7 - w) * 1024 + lane * 16) = o;
}
DI void u_store(char* smem, char* ws, int w, int lane, int tb) {
  const char* p = smem + K1_LN + ((lane & 15) >> 2) * 1024 + (64 * w + (lane >> 4) * 16 + (lane & 3) * 4) * 2;
  const u32x2 a = tr_rd(p), c = tr_rd(p + 4096);
  *(u32x4*)(ws + WS_UT + (size_t)tb * 8192 + (64 * w + lane) * 16) = (u32x4){a[0], a[1], c[0], c[1]};
}

__global__ __launch_bounds__(512, 2) void k1_ffn_up(const u16* __restrict__ x, const u16* __restrict__ w1,
                                                   const u16* __restrict__ b1, const u16* __restrict__ w2,
                                                   char* __restrict__ ws) {
  extern __shared__ __attribute__((aligned(16))) char smem[];
  const int tid = threadIdx.x, lane = tid & 63;
  const int w = __builtin_amdgcn_readfirstlane(tid >> 6);
  const int b = blockIdx.x, xcd = b & 7, jj = b >> 3;
  const int rb = xcd * 4 + (jj & 3), cb = jj >> 2;
  const int r = lane & 31, h = lane >> 5;
  const unsigned lds0 = (unsigned)(size_t)smem;
  ST_DECL; ST();
  const char* xb = (const char*)x;
  const char* wb = (const char*)w1;
  const int tb = rb * 8 + cb;
#define LOAD_ROW() \
  u32x4 xr; u32x2 ep; \
  { const unsigned vo = (unsigned)((tb * 8 + w) * 1024 + lane * 16); const char* epp = ws + WS_EP + rb * 128; const unsigned z = 0; \
    asm volatile("global_load_dwordx4 %0, %2, %3\n\tglobal_load_dwordx2 %1, %4, %5 sc0 sc1" \
                 : "=&v"(xr), "=&v"(ep) : "v"(vo), "s"(xb), "v"(z), "s"(epp) : "memory"); }
  unsigned vx;
  {
    const int tk = w * 8 + (lane >> 3);
    vx = (unsigned)((rb * 64 + tk) * 1024 + (((lane & 7) ^ ((tk >> 1) & 7)) << 4));
  }
  const unsigned ldx = lds0 + w * 1024;
  const int blk = (lane >> 4) & 1, q4 = (lane & 15) >> 2, p4 = lane & 3;

  if (w < 7) {
    LOAD_ROW();
    unsigned vw[4];
#pragma unroll
    for (int m = 0; m < 4; ++m) {
      int c = (4 * w + m) * 64 + lane, row = c / 28, cc = c - row * 28;
      vw[m] = (unsigned)(row * 3584 + cb * 448 + cc * 16 - m * 1024);
    }
    const unsigned ldw = lds0 + 8192 + w * 4096;
#pragma unroll
    for (int s = 0; s < 4; ++s) {
      dma_x(ldx + s * STG, xb + s * 128, vx);
      dma_w(ldw + s * STG, wb + (size_t)s * 64 * 3584, vw);
    }
    asm volatile("s_waitcnt vmcnt(20)" : "+v"(xr), "+v"(ep)::"memory");
    ln_row(xr, ep, smem, ws, w, lane, tb * 8 + w);
    const unsigned aoff = 8192 + (8 * h + q4) * 448 + (w * 32 + 16 * blk + 4 * p4) * 2;
    unsigned boff[4];
#pragma unroll
    for (int k = 0; k < 4; ++k) boff[k] = r * 128 + (((2 * k + h) ^ ((r >> 1) & 7)) << 4);
    f32x16 acc[2];
#pragma unroll
    for (int i = 0; i < 16; ++i) { acc[0][i] = 0.f; acc[1][i] = 0.f; }
    u32x4 A[2][4], B0[2][4], B1[2][4];
    u16 b1v;
    float cs;
    unsigned ms0, ms1;
#pragma unroll
    for (int s = 0; s <= 8; ++s) {
      if (s == 0) WAIT_VM(16); else if (s <= 3) WAIT_VM(12); else if (s <= 5) WAIT_VM(10); else if (s == 6) WAIT_VM(5); else if (s == 7) WAIT_VM(0);
      BARRIER();
      if (s == 0 || s == 1 || s == 4 || s == 7) ST();
      const char* sb = smem + (s & 3) * STG;
      const int p = (s - 1) & 1;
      const bool dm = (s >= 1 && s + 3 <= 7);
      if (s == 8) {
        cs = *(const float*)(smem + K1_CS + (w * 32 + r) * 4);
        ms0 = *(const unsigned*)(smem + K1_MS + r * 4);
        ms1 = *(const unsigned*)(smem + K1_MS + 128 + r * 4);
      }
#pragma unroll
      for (int k = 0; k < 4; ++k) {
        if (s >= 1) acc[0] = __builtin_amdgcn_mfma_f32_32x32x16_bf16(as_bf8(A[p][k]), as_bf8(B0[p][k]), acc[0], 0, 0, 0);
        SCHED();
        if (s < 8) {
          const u32x2 al = tr_rd(sb + aoff + (16 * k) * 448), ah = tr_rd(sb + aoff + (16 * k + 4) * 448);
          A[s & 1][k] = (u32x4){al[0], al[1], ah[0], ah[1]};
          B0[s & 1][k] = *(const u32x4*)(sb + boff[k]);
          B1[s & 1][k] = *(const u32x4*)(sb + 4096 + boff[k]);
        }
        SCHED();
        if (s >= 1) acc[1] = __builtin_amdgcn_mfma_f32_32x32x16_bf16(as_bf8(A[p][k]), as_bf8(B1[p][k]), acc[1], 0, 0, 0);
        SCHED();
        if (dm) {
          if (k == 0) dma_x(ldx + ((s + 3) & 3) * STG, xb + (s + 3) * 128, vx);
          if (k == 1) dma_w2(ldw + ((s + 3) & 3) * STG, wb + (size_t)(s + 3) * 64 * 3584, vw[0], vw[1]);
          if (k == 2) dma_w2(ldw + ((s + 3) & 3) * STG + 2048, wb + (size_t)(s + 3) * 64 * 3584 + 2048, vw[2], vw[3]);
        }
        SCHED();
      }
      if (s == 0) u_store(smem, ws, w, lane, tb);
      if (s == 7) b1v = b1[cb * 224 + w * 32 + r];
      SCHED();
    }
    const float hi = rbf(cs), r1 = cs - hi, mid = rbf(r1), lo = r1 - mid;
    u32x4 a = {pk_bf16(hi, mid), bfbits(lo) | ((unsigned)b1v << 16), 0u, 0u};
    if (h) { a[0] = 0u; a[1] = 0u; }
    const unsigned ms[2] = {ms0, ms1};
#pragma unroll
    for (int t = 0; t < 2; ++t) {
      const unsigned nm = (ms[t] & 0xffffu) ^ 0x8000u;
      const u32x4 bb = {nm | (nm << 16), nm | (ms[t] & 0xffff0000u), 0u, 0u};
      acc[t] = __builtin_amdgcn_mfma_f32_32x32x16_bf16(as_bf8(a), as_bf8(bb), acc[t], 0, 0, 0);
    }
    ST();
#pragma unroll
    for (int t = 0; t < 2; ++t) {
      const float sdv = bfhi(ms[t]), rho = __builtin_amdgcn_rcpf(sdv);
      const float ca = -2.302208198f * rho, cbb = -0.1029432397f * rho * rho * rho;
      char* hp = ws + WS_HF + ((size_t)((rb * 2 + t) * 112 + cb * 14 + 2 * w) * 64 + lane) * 16;
#pragma unroll
      for (int hh = 0; hh < 2; ++hh) {
        unsigned o[4];
#pragma unroll
        for (int i = 0; i < 4; ++i) {
          float g[2];
#pragma unroll
          for (int e = 0; e < 2; ++e) {
            float d = acc[t][8 * hh + 2 * i + e];
            float ex = __builtin_amdgcn_exp2f(d * fmaf(d * d, cbb, ca));
            g[e] = d * __builtin_amdgcn_rcpf(fmaf(ex, sdv, sdv));
          }
          o[i] = pk_bf16(g[0], g[1]);
        }
        *(u32x4*)(hp + hh * 1024) = (u32x4){o[0], o[1], o[2], o[3]};
      }
    }
  } else {
    LOAD_ROW();
#pragma unroll
    for (int s = 0; s < 4; ++s) dma_x(ldx + s * STG, xb + s * 128, vx);
    SCHED();
    unsigned w2v[7][8];
    {
      const auto rw = __builtin_amdgcn_make_buffer_rsrc((void*)w2, 0, F_ * D_ * 2, 0x00020000);
      const unsigned vo = h * 4096 + r * 2;
#pragma unroll
      for (int f = 0; f < 7; ++f) {
        const int gw = b * 7 + f, dt = gw / 112, ks = gw - dt * 112;
        const unsigned so = ks * 16384 + dt * 64;
#pragma unroll
        for (int j = 0; j < 8; ++j)
          w2v[f][j] = __builtin_amdgcn_raw_buffer_load_b16(rw, vo + (j & 3) * 1024, so + (j >> 2) * 8192, 0);
      }
    }
    SCHED();
    asm volatile("s_waitcnt vmcnt(60)" : "+v"(xr), "+v"(ep)::"memory");
    ln_row(xr, ep, smem, ws, w, lane, tb * 8 + w);
    const int row16 = lane & 15;
    const unsigned one2 = 0x3F803F80u;
    const u32x4 sa = {row16 < 2 ? one2 : 0u, row16 < 2 ? one2 : 0u, (row16 & 14) == 2 ? one2 : 0u, (row16 & 14) == 2 ? one2 : 0u};
    const int sidx = (lane & 1) ? 0xEEEEEEEE : 0x44444444;
    const unsigned aoff = 8192 + (8 * h + q4) * 448 + (16 * blk + 4 * p4) * 2;
    f32x4 ca[7];
#pragma unroll
    for (int g = 0; g < 7; ++g) ca[g] = (f32x4){0.f, 0.f, 0.f, 0.f};
    const auto rs = __builtin_amdgcn_make_buffer_rsrc((void*)ws, 0, WS_END, 0x00020000);
    const unsigned goff = (unsigned)WS_GR + (rb * 64 + lane) * 16;
    u32x4 gr;
    u32x8 bb[14];
#pragma unroll
    for (int s = 0; s < 8; ++s) {
      if (s == 0) WAIT_VM(60); else if (s <= 3) WAIT_VM(60); else if (s <= 5) WAIT_VM(2); else if (s == 6) WAIT_VM(1); else WAIT_VM(0);
      BARRIER();
      if (s == 0 || s == 1 || s == 4 || s == 7) ST();
      if (s >= 1 && s + 3 <= 7) dma_x(ldx + ((s + 3) & 3) * STG, xb + (s + 3) * 128, vx);
      if (s == 7) gr = __builtin_amdgcn_raw_buffer_load_b128(rs, goff, 0, 16);
      if (s == 0) u_store(smem, ws, w, lane, tb);
      unsigned ab = lds0 + aoff + (s & 3) * STG;
      asm volatile("" : "+v"(ab));
      const LAS char* sp = (const LAS char*)(size_t)ab;
#pragma unroll
      for (int q = 0; q < 14; ++q) {
        const LAS char* p = sp + (q >> 1) * 64 + (32 * (q & 1)) * 448;
        const u32x2 t0 = tr_rd3(p), t1 = tr_rd3(p + 4 * 448), t2 = tr_rd3(p + 16 * 448), t3 = tr_rd3(p + 20 * 448);
        bb[q] = (u32x8){t0[0], t0[1], t1[0], t1[1], t2[0], t2[1], t3[0], t3[1]};
      }
      SCHED();
#pragma unroll
      for (int q = 0; q < 14; ++q)
        ca[q >> 1] = __builtin_amdgcn_smfmac_f32_16x16x64_bf16(as_bf8(sa), __builtin_bit_cast(bf16x16, bb[q]), ca[q >> 1], sidx, 0, 0);
      SCHED();
    }
    if (lane < 16) {
#pragma unroll
      for (int g = 0; g < 7; ++g) {
        *(float*)(smem + K1_CS + (g * 32 + lane) * 4) = ca[g][0] + ca[g][1];
        *(float*)(smem + K1_CS + (g * 32 + 16 + lane) * 4) = ca[g][2] + ca[g][3];
      }
    }
    {
      const unsigned long long e1 = (((unsigned long long)ep[1] << 32) | ep[0]) + 1ull;
      const unsigned tl = (unsigned)(e1 << 16), th = (unsigned)(e1 >> 16);
      for (unsigned spins = 0; spins < (1u << 20); ++spins) {
        const bool ok = (gr[0] & 0xffff0000u) == tl && gr[1] == th && (gr[2] & 0xffff0000u) == tl && gr[3] == th;
        if (__all(ok)) break;
        __builtin_amdgcn_s_sleep(2);
        gr = __builtin_amdgcn_raw_buffer_load_b128(rs, goff, 0, 16);
      }
      *(unsigned*)(smem + K1_MS + lane * 4) = (gr[0] & 0xffffu) | (gr[2] << 16);
      BARRIER();
      if (cb == 0 && lane == 0)
        __hip_atomic_store((GAS unsigned long long*)(ws + WS_EP + rb * 128), e1, __ATOMIC_RELAXED, __HIP_MEMORY_SCOPE_AGENT);
    }
    ST();
#pragma unroll
    for (int f = 0; f < 7; ++f) {
      asm volatile("" : "+v"(w2v[f][0]), "+v"(w2v[f][1]), "+v"(w2v[f][2]), "+v"(w2v[f][3]), "+v"(w2v[f][4]), "+v"(w2v[f][5]),
                   "+v"(w2v[f][6]), "+v"(w2v[f][7]));
      u32x4 v = {w2v[f][0] | (w2v[f][1] << 16), w2v[f][2] | (w2v[f][3] << 16), w2v[f][4] | (w2v[f][5] << 16),
                 w2v[f][6] | (w2v[f][7] << 16)};
      *(u32x4*)(ws + WS_W2F + (size_t)(b * 7 + f) * 1024 + lane * 16) = v;
    }
    {
      const u32x4 sent = {~0u, ~0u, ~0u, ~0u};
      char* sp = ws + WS_ST + (size_t)b * 8192 + lane * 16;
#pragma unroll
      for (int i = 0; i < 8; ++i) *(u32x4*)(sp + i * 1024) = sent;
    }
  }
  ST();
}

constexpr int CP_STRIDE = 4352;
constexpr int CV_CP = 0;
constexpr int CV_TAB = 53 * 272;
constexpr int CV_U = 8 * CP_STRIDE;
constexpr int CV_XK = CV_U + 4608;
constexpr int CV_XY = CV_XK + 6144;
constexpr int CV_BYTES = CV_XY + 8192;
constexpr int K2_FLAG = 2 * CV_BYTES;
constexpr int K2_LDS = K2_FLAG + 64;
#define CP_OFF(sig) (((0xDC885410u >> (4 * (sig))) & 15u) * 16u + (sig) * CP_STRIDE)

DI void lds_set(LAS unsigned* p, unsigned v) {
  WAIT_LGKM0();
  __hip_atomic_store(p, v, __ATOMIC_RELAXED, __HIP_MEMORY_SCOPE_WORKGROUP);
  asm volatile("" ::: "memory");
}
DI void lds_wait(LAS unsigned* p, unsigned v) {
  while ((unsigned)__builtin_amdgcn_readfirstlane(__hip_atomic_load(p, __ATOMIC_RELAXED, __HIP_MEMORY_SCOPE_WORKGROUP)) < v)
    __builtin_amdgcn_s_sleep(0);
  asm volatile("" ::: "memory");
}
DI f32x2 cmul(f32x2 a, f32x2 b, f32x2 bn) {
  f32x2 r2 = {a[0], a[0]}, i2 = {a[1], a[1]};
  return __builtin_elementwise_fma(i2, bn, r2 * b);
}
DI f32x2 rot(f32x2 b) { return (f32x2){-b[1], b[0]}; }
DI f32x2 cexp_p(float zr2, float zi, float p) {
  float th = zi * p;
  float n = __builtin_rintf(th * 0.15915494f);
  float rr = fmaf(th, 0.15915494f, -n);
  rr = fmaf(th, 6.4206382e-9f, rr);
  float mag = __builtin_amdgcn_exp2f(zr2 * p);
  return (f32x2){mag * __builtin_amdgcn_cosf(rr), mag * __builtin_amdgcn_sinf(rr)};
}

__global__ __launch_bounds__(512, 2) void k2_down_conv(const u16* __restrict__ x, const u16* __restrict__ lar,
                                                      const u16* __restrict__ aim, const u16* __restrict__ cre,
                                                      const u16* __restrict__ cim, const u16* __restrict__ ldt,
                                                      const u16* __restrict__ dskip, const u16* __restrict__ b2,
                                                      char* __restrict__ ws, u16* __restrict__ out) {
  extern __shared__ __attribute__((aligned(16))) char smem[];
  const int tid = threadIdx.x, lane = tid & 63;
  const int w = __builtin_amdgcn_readfirstlane(tid >> 6);
  const int b = blockIdx.x, xcd = b & 7, jj = b >> 3;
  const int rb = xcd * 4 + (jj & 3), cb = jj >> 2;
  const int r = lane & 31, h = lane >> 5;
  LAS unsigned* flags = (LAS unsigned*)(smem + K2_FLAG);
  if (tid < 16) flags[tid] = 0u;
  ST_DECL; ST();
  const int cw = w & 3, chl = cw >> 1, hf = cw & 1;
  const int c = 2 * (xcd * 32 + jj) + chl, n = hf * 64 + lane;
  char* cv = smem + chl * CV_BYTES;
  char* tab = cv + hf * CV_TAB + lane * 4;
  const u16 p_ldt = ldt[c], p_lar = lar[c * N_ + n], p_aim = aim[c * N_ + n];
  const auto rs = __builtin_amdgcn_make_buffer_rsrc((void*)ws, 0, WS_END, 0x00020000);
  const unsigned s_off = (unsigned)WS_ST + (cb * 64 + lane) * 4096 + (rb * 64 + w * 8) * 2;

  u16 xv[8];
  u16 b2v;
  u32x4 sv;
#define PUT(row, v) *(unsigned*)(tab + (row) * 272) = pk_f16v(v)

  if (w < 4) {
    SCHED();
    BARRIER();
    const char* ha = ws + WS_HF + ((size_t)((rb * 2) * 112 + 28 * w) * 64 + lane) * 16;
    const char* wbp = ws + WS_W2F + ((size_t)((cb * 2) * 112 + 28 * w) * 64 + lane) * 16;
    constexpr int P = 8, P0 = 4;
    u32x4 fa[P][2], fb[P][2];
#pragma unroll
    for (int k = 0; k < P0; ++k) {
      fa[k][0] = *(const u32x4*)(ha + k * 1024);
      fb[k][0] = *(const u32x4*)(wbp + k * 1024);
      fa[k][1] = *(const u32x4*)(ha + 112 * 1024 + k * 1024);
      fb[k][1] = *(const u32x4*)(wbp + 112 * 1024 + k * 1024);
      SCHED();
    }
    {
      const float dt = rbf(expf(bf2f(p_ldt)));
      const float zr2 = -rbf(expf(bf2f(p_lar))) * dt * 1.44269504f, zi = bf2f(p_aim) * dt;
      const f32x2 w8 = cexp_p(zr2, zi, 8.f), w8n = rot(w8);
      f32x2 E = cexp_p(zr2, zi, 64.f), E2 = cexp_p(zr2, zi, 160.f);
      PUT(21 + 8, E); PUT(21 + 20, E2);
#pragma unroll
      for (int k = 1; k < 12; ++k) {
        E = cmul(E, w8, w8n); E2 = cmul(E2, w8, w8n);
        PUT(21 + 8 + k, E); PUT(21 + 20 + k, E2);
      }
      lds_set(flags + 12 + cw, 1u);
    }
    SCHED();
#pragma unroll
    for (int k = P0; k < P; ++k) {
      fa[k][0] = *(const u32x4*)(ha + k * 1024);
      fb[k][0] = *(const u32x4*)(wbp + k * 1024);
      fa[k][1] = *(const u32x4*)(ha + 112 * 1024 + k * 1024);
      fb[k][1] = *(const u32x4*)(wbp + 112 * 1024 + k * 1024);
      SCHED();
    }
    ST();
    f32x16 acc[2][2];
#pragma unroll
    for (int i = 0; i < 16; ++i) { acc[0][0][i] = 0.f; acc[0][1][i] = 0.f; acc[1][0][i] = 0.f; acc[1][1][i] = 0.f; }
#pragma unroll
    for (int k = 0; k < 28; ++k) {
      const int sl = k % P;
      u32x4 a0 = fa[sl][0], a1 = fa[sl][1], b0 = fb[sl][0], b1 = fb[sl][1];
      acc[0][0] = __builtin_amdgcn_mfma_f32_32x32x16_bf16(as_bf8(a0), as_bf8(b0), acc[0][0], 0, 0, 0);
      acc[0][1] = __builtin_amdgcn_mfma_f32_32x32x16_bf16(as_bf8(a0), as_bf8(b1), acc[0][1], 0, 0, 0);
      acc[1][0] = __builtin_amdgcn_mfma_f32_32x32x16_bf16(as_bf8(a1), as_bf8(b0), acc[1][0], 0, 0, 0);
      acc[1][1] = __builtin_amdgcn_mfma_f32_32x32x16_bf16(as_bf8(a1), as_bf8(b1), acc[1][1], 0, 0, 0);
      SCHED();
      if (k == 0 || k == 8 || k == 16) ST();
      if (k + P < 28) {
        fa[sl][0] = *(const u32x4*)(ha + (k + P) * 1024);
        fb[sl][0] = *(const u32x4*)(wbp + (k + P) * 1024);
        fa[sl][1] = *(const u32x4*)(ha + 112 * 1024 + (k + P) * 1024);
        fb[sl][1] = *(const u32x4*)(wbp + 112 * 1024 + (k + P) * 1024);
      }
      if (k + P == 28) {
#pragma unroll
        for (int j = 0; j < 8; ++j) xv[j] = x[(size_t)(rb * 64 + w * 8 + j) * D_ + cb * 64 + lane];
        b2v = b2[cb * 64 + lane];
      }
      if (k == 25) sv = __builtin_amdgcn_raw_buffer_load_b128(rs, s_off, 0, 16);
      SCHED();
    }
    ST();
    lds_wait(flags + 8, 4u);
#pragma unroll
    for (int a = 0; a < 2; ++a)
#pragma unroll
      for (int bb = 0; bb < 2; ++bb)
#pragma unroll
        for (int g = 0; g < 4; ++g) {
          const int d = 32 * bb + r, ch = 8 * a + 2 * g + h;
          f32x4 v = {acc[a][bb][4 * g], acc[a][bb][4 * g + 1], acc[a][bb][4 * g + 2], acc[a][bb][4 * g + 3]};
          *(f32x4*)(smem + w * 16384 + d * 256 + ((ch ^ (d & 15)) << 4)) = v;
        }
  } else {
    const u16 p_cre = cre[c * N_ + n], p_cim = cim[c * N_ + n], p_ds = dskip[c];
    const char* ug = ws + WS_UT + (size_t)(255 - 128 * hf - lane) * 8192 + c * 16;
    const u32x4 u0 = *(const u32x4*)ug, u1 = *(const u32x4*)(ug - 64 * 8192);
    __builtin_amdgcn_s_setprio(3);
    SCHED();
    BARRIER();

    const float dt = rbf(expf(bf2f(p_ldt)));
    const float zr2 = -rbf(expf(bf2f(p_lar))) * dt * 1.44269504f, zi = bf2f(p_aim) * dt;
    f32x2 cc = {bf2f(p_cre) * dt, -bf2f(p_cim) * dt};
    const int ce = __builtin_amdgcn_frexp_expf(wave_absmax(fmaxf(fabsf(cc[0]), fabsf(cc[1]))));
    cc *= __builtin_amdgcn_ldexpf(1.f, -ce);
    const float unsc = __builtin_amdgcn_ldexpf(2.f, ce);
    {
      const f32x2 e1 = cexp_p(zr2, -zi, 1.f), w8 = cexp_p(zr2, zi, 8.f), G = cexp_p(zr2, -zi, 256.f);
      const f32x2 e1n = rot(e1), w8n = rot(w8), Gn = rot(G);
      const f32x2 e2 = cmul(e1, e1, e1n), e2n = rot(e2), e4 = cmul(e2, e2, e2n), e4n = rot(e4);
      f32x2 R = cc, E = w8, Gq = cmul(cc, G, Gn), T = Gq;
      PUT(0, R); *(unsigned*)(tab + 21 * 272) = 0x00003C00u; PUT(22, E); PUT(8, Gq);
#pragma unroll
      for (int k = 1; k < 8; ++k) {
        R = cmul(R, e1, e1n); PUT(k, R);
        Gq = cmul(Gq, G, Gn);
        if (k == 1) { PUT(12, Gq); f32x2 q = cmul(Gq, e4, e4n); PUT(13, q); }
        else if (k == 2) { PUT(14, Gq); f32x2 q = cmul(Gq, e4, e4n); PUT(15, q); }
        else PUT(13 + k, Gq);
        if (k < 4) { T = cmul(T, e2, e2n); PUT(8 + k, T); }
        if (k < 7) { E = cmul(E, w8, w8n); PUT(22 + k, E); }
      }
    }
    ST();
    *(u32x4*)(cv + CV_U + hf * 2048 + lane * 16) = u0;
    *(u32x4*)(cv + CV_U + hf * 2048 + 1024 + lane * 16) = u1;
    if (lane < 16) *(u32x4*)(cv + CV_U + 4096 + hf * 256 + lane * 16) = (u32x4){0u, 0u, 0u, 0u};
#pragma unroll
    for (int j = 0; j < 8; ++j) xv[j] = x[(size_t)(rb * 64 + w * 8 + j) * D_ + cb * 64 + lane];
    b2v = b2[cb * 64 + lane];
    lds_wait(flags + 12 + cw, 1u);
    float kx[12];
    {
      f32x16 ka;
#pragma unroll
      for (int i = 0; i < 16; ++i) ka[i] = 0.f;
      const char* tb = cv + hf * CV_TAB + r * 272 + h * 16;
      u32x4 ta[8], tc[8];
#pragma unroll
      for (int k = 0; k < 8; ++k) { ta[k] = *(const u32x4*)(tb + k * 32); tc[k] = *(const u32x4*)(tb + 21 * 272 + k * 32); }
#pragma unroll
      for (int k = 0; k < 8; ++k) ka = __builtin_amdgcn_mfma_f32_32x32x16_f16(as_h8(ta[k]), as_h8(tc[k]), ka, 0, 0, 0);
#pragma unroll
      for (int i = 0; i < 12; ++i) kx[i] = ka[i] * unsc;
      char* xk = cv + CV_XK + hf * 3072 + lane * 16;
#pragma unroll
      for (int g = 0; g < 3; ++g) *(f32x4*)(xk + g * 1024) = (f32x4){kx[4 * g], kx[4 * g + 1], kx[4 * g + 2], kx[4 * g + 3]};
      lds_set(flags + cw, 1u);
      lds_wait(flags + (cw ^ 1), 1u);
      const char* xo = cv + CV_XK + (hf ^ 1) * 3072 + lane * 16;
#pragma unroll
      for (int g = 0; g < 3; ++g) {
        f32x4 o = *(const f32x4*)(xo + g * 1024);
#pragma unroll
        for (int e = 0; e < 4; ++e) kx[4 * g + e] += o[e];
      }
    }
    ST();
    float km = 0.f, km2 = 0.f;
#pragma unroll
    for (int i = 0; i < 8; ++i) km = fmaxf(km, fabsf(kx[i]));
#pragma unroll
    for (int i = 9; i < 12; ++i) km2 = fmaxf(km2, fabsf(kx[i]));
    km = fmaxf(km, h ? 0.f : km2);
    km = fmaxf(km, lane <= 32 ? fabsf(kx[8]) : 0.f);
    const int ke = __builtin_amdgcn_frexp_expf(wave_absmax(km));
    const float ksc = __builtin_amdgcn_ldexpf(1.f, -ke), kun = __builtin_amdgcn_ldexpf(1.f, ke);
#pragma unroll
    for (int i = 0; i < 12; ++i) kx[i] *= ksc;
    {
      char* c0 = cv + CV_CP + 32;
      const bool last = (r == 31);
      const float f512 = rdlane(kx[4], 32), f768 = rdlane(kx[6], 32), f1024 = rdlane(kx[8], 0), f1280 = rdlane(kx[9], 0),
                  f1536 = rdlane(kx[10], 0), f1792 = rdlane(kx[11], 0), f2048 = rdlane(kx[8], 32);
      const float n4 = next_lane(kx[4]), n6 = next_lane(kx[6]);
      *(u32x2*)(c0 + (8 * r + 4 * h) * 2) = (u32x2){pk_f16(kx[0], kx[1]), pk_f16(kx[2], kx[3])};
      {
        const float nn = last ? (h ? f768 : f512) : (h ? n4 : n4);
        const float e1_ = h ? kx[4] : kx[5], e2_ = h ? kx[5] : kx[6], e3_ = h ? nn : kx[7];
        *(u32x4*)(c0 + (256 + 256 * h + 8 * r) * 2) =
            (u32x4){pk_f16(kx[4], kx[4]), pk_f16(e1_, e2_), pk_f16(e2_, e2_), pk_f16(e3_, nn)};
      }
      const float n8 = next_lane(kx[8]), n9 = next_lane(kx[9]), n10 = next_lane(kx[10]), n11 = next_lane(kx[11]);
      {
        const float k0 = h ? kx[6] : kx[8];
        const float nn = last ? (h ? f1024 : f1280) : (h ? n6 : n8);
        const float e1_ = h ? kx[7] : k0, e2_ = h ? kx[7] : ((r & 1) ? nn : k0), e3_ = h ? kx[7] : nn;
        *(u32x4*)(c0 + (h ? 768 + 8 * r : 1024 + 8 * r) * 2) = (u32x4){pk_f16(k0, k0), pk_f16(k0, e1_), pk_f16(e2_, e3_), pk_f16(nn, nn)};
      }
      if (h == 0) {
        const float fl[3] = {f1536, f1792, f2048}, nx[3] = {n9, n10, n11};
#pragma unroll
        for (int a = 0; a < 3; ++a) {
          const float k0 = kx[9 + a], nn = last ? fl[a] : nx[a];
          const unsigned kk = pk_f16(k0, k0), n2 = pk_f16(nn, nn);
          *(u32x4*)(c0 + (1280 + 256 * a + 8 * r) * 2) = (u32x4){kk, kk, (r & 1) ? n2 : pk_f16(k0, nn), n2};
        }
      } else if (r < 2) *(u32x4*)(cv + CV_CP + r * 16) = (u32x4){0u, 0u, 0u, 0u};
    }
    WAIT_LGKM0();
    {
      u32x4 lo[3], hi[3];
      int chs[3];
#pragma unroll
      for (int rd = 0; rd < 3; ++rd) {
        const int idx = min(rd * 64 + lane, 143);
        const int k1 = idx / 18;
        chs[rd] = 16 * hf + (idx - 18 * k1) + 32 * k1;
        lo[rd] = *(const u32x4*)(cv + CV_CP + chs[rd] * 16);
        hi[rd] = *(const u32x4*)(cv + CV_CP + chs[rd] * 16 + 16);
      }
#pragma unroll
      for (int rd = 0; rd < 3; ++rd) {
        const unsigned d[8] = {lo[rd][0], lo[rd][1], lo[rd][2], lo[rd][3], hi[rd][0], hi[rd][1], hi[rd][2], hi[rd][3]};
        unsigned al[7];
#pragma unroll
        for (int i = 0; i < 7; ++i) al[i] = __builtin_amdgcn_alignbit(d[i + 1], d[i], 16);
        char* dst = cv + CV_CP + chs[rd] * 16;
        if (rd < 2 || lane < 16) {
#pragma unroll
          for (int m = 0; m < 4; ++m) {
            if (m) *(u32x4*)(dst + CP_OFF(2 * m)) = (u32x4){d[m], d[m + 1], d[m + 2], d[m + 3]};
            *(u32x4*)(dst + CP_OFF(2 * m + 1)) = (u32x4){al[m], al[m + 1], al[m + 2], al[m + 3]};
          }
        }
      }
    }
    WAIT_LGKM0();
    ST();
    const int i16 = lane & 15, g4 = lane >> 4, a1 = i16 + 1;
    const char* ap = cv + CV_CP + CP_OFF(a1 & 7) + (16 * hf + g4 + (a1 >> 3)) * 16;
    const char* bp = cv + CV_U + (2032 + 128 * hf - 16 * i16 + 8 * g4) * 2;
    f32x4 ya[8];
#pragma unroll
    for (int t = 0; t < 8; ++t) ya[t] = (f32x4){0.f, 0.f, 0.f, 0.f};
    {
      u32x4 A[2][8], B[2][8];
#pragma unroll
      for (int k0 = 0; k0 <= 4; ++k0) {
        if (k0 < 4) {
#pragma unroll
          for (int i = 0; i < 8; ++i) {
            A[k0 & 1][i] = *(const u32x4*)(ap + k0 * 64 + i * 512);
            B[k0 & 1][i] = *(const u32x4*)(bp + k0 * 64 - i * 512);
          }
          SCHED();
        }
        if (k0 >= 1) {
          const int p = (k0 - 1) & 1;
#pragma unroll
          for (int dl = 0; dl < 8; ++dl)
#pragma unroll
            for (int k1 = 0; k1 + dl < 8; ++k1)
              ya[k1 + dl] = __builtin_amdgcn_mfma_f32_16x16x32_f16(as_h8(A[p][k1]), as_h8(B[p][dl]), ya[k1 + dl], 0, 0, 0);
          SCHED();
        }
      }
    }
    ST();
    {
      char* xy = cv + CV_XY + hf * 4096 + lane * 16;
#pragma unroll
      for (int t = 0; t < 8; ++t) asm volatile("" : "+v"(ya[t]));
      f32x4 mine[4];
      if (hf) {
#pragma unroll
        for (int t = 0; t < 4; ++t) { *(f32x4*)(xy + t * 1024) = ya[t]; mine[t] = ya[4 + t]; }
      } else {
#pragma unroll
        for (int t = 0; t < 4; ++t) { *(f32x4*)(xy + t * 1024) = ya[4 + t]; mine[t] = ya[t]; }
      }
      lds_set(flags + 4 + cw, 1u);
      const float dsk = bf2f(p_ds);
      const int tb0 = 1024 * hf + 16 * i16 + 4 * g4;
      float xd[4][4];
#pragma unroll
      for (int t = 0; t < 4; ++t) {
        const u32x2 uu = *(const u32x2*)(cv + CV_U + (2044 - tb0 - 256 * t) * 2);
        const f16x2 ua = as_h2(uu[0]), ub = as_h2(uu[1]);
        const unsigned p0 = pk_bf16((float)ub[1] * dsk, (float)ub[0] * dsk), p1 = pk_bf16((float)ua[1] * dsk, (float)ua[0] * dsk);
        xd[t][0] = bflo(p0); xd[t][1] = bfhi(p0); xd[t][2] = bflo(p1); xd[t][3] = bfhi(p1);
      }
      lds_wait(flags + 4 + (cw ^ 1), 1u);
      const char* xo = cv + CV_XY + (hf ^ 1) * 4096 + lane * 16;
#pragma unroll
      for (int t = 0; t < 4; ++t) {
        const f32x4 y = (*(const f32x4*)(xo + t * 1024) + mine[t]) * kun;
        const unsigned y0 = pk_bf16(y[0], y[1]), y1 = pk_bf16(y[2], y[3]);
        unsigned s0 = pk_bf16(bflo(y0) + xd[t][0], bfhi(y0) + xd[t][1]);
        const unsigned s1 = pk_bf16(bflo(y1) + xd[t][2], bfhi(y1) + xd[t][3]);
        if ((s0 & 0xffffu) == 0xffffu) s0 ^= 0x3fu;
        __hip_atomic_store((GAS unsigned long long*)(ws + WS_ST + (size_t)c * 4096 + (tb0 + 256 * t) * 2),
                           (unsigned long long)s0 | ((unsigned long long)s1 << 32), __ATOMIC_RELAXED, __HIP_MEMORY_SCOPE_AGENT);
      }
    }
    WAIT_LGKM0();
    if (lane == 0) __hip_atomic_fetch_add(flags + 8, 1u, __ATOMIC_RELAXED, __HIP_MEMORY_SCOPE_WORKGROUP);
    ST();
    sv = __builtin_amdgcn_raw_buffer_load_b128(rs, s_off, 0, 16);
  }
  for (unsigned spins = 0; spins < (1u << 20); ++spins) {
    const bool bad = ((sv[0] & 0xffffu) == 0xffffu) || ((sv[2] & 0xffffu) == 0xffffu);
    if (!__any(bad)) break;
    __builtin_amdgcn_s_sleep(4);
    sv = __builtin_amdgcn_raw_buffer_load_b128(rs, s_off, 0, 16);
  }
  ST();
  BARRIER();
  ST();
  {
    f32x4 v0 = {0.f, 0.f, 0.f, 0.f}, v1 = {0.f, 0.f, 0.f, 0.f};
#pragma unroll
    for (int q = 0; q < 4; ++q) {
      v0 += *(const f32x4*)(smem + q * 16384 + lane * 256 + (((2 * w) ^ (lane & 15)) << 4));
      v1 += *(const f32x4*)(smem + q * 16384 + lane * 256 + (((2 * w + 1) ^ (lane & 15)) << 4));
    }
    const float bb = bf2f(b2v);
    u16* op = out + (size_t)(rb * 64 + w * 8) * D_ + cb * 64 + lane;
#pragma unroll
    for (int j = 0; j < 4; ++j) {
      const float a0 = j < 2 ? v0[2 * j] : v1[2 * j - 4], a1 = j < 2 ? v0[2 * j + 1] : v1[2 * j - 3];
      const unsigned pa = pk_bf16(a0, a1);
      const unsigned pv = pk_bf16(bflo(pa) + bb, bfhi(pa) + bb);
      const float q0 = bflo(pv), q1 = bfhi(pv);
      const unsigned pg = pk_bf16(q0 * __builtin_amdgcn_rcpf(1.f + __builtin_amdgcn_exp2f(q0 * -1.44269504f)),
                                  q1 * __builtin_amdgcn_rcpf(1.f + __builtin_amdgcn_exp2f(q1 * -1.44269504f)));
      const unsigned ps = pk_bf16(bflo(sv[j]) * bflo(pg), bfhi(sv[j]) * bfhi(pg));
      const unsigned po = pk_bf16(bf2f(xv[2 * j]) + bflo(ps), bf2f(xv[2 * j + 1]) + bfhi(ps));
      op[(size_t)(2 * j) * D_] = (u16)po;
      op[(size_t)(2 * j + 1) * D_] = (u16)(po >> 16);
    }
  }
}

extern "C" void kernel_launch(void* const* d_in, const int* in_sizes, int n_in, void* d_out, int out_size, void* d_ws,
                              size_t ws_size, hipStream_t stream) {
  (void)in_sizes; (void)n_in; (void)out_size; (void)ws_size;
  (void)hipFuncSetAttribute((const void*)k1_ffn_up, hipFuncAttributeMaxDynamicSharedMemorySize, K1_LDS);
  (void)hipFuncSetAttribute((const void*)k2_down_conv, hipFuncAttributeMaxDynamicSharedMemorySize, K2_LDS);
  const u16* x = (const u16*)d_in[0];
  k1_ffn_up<<<256, 512, K1_LDS, stream>>>(x, (const u16*)d_in[8], (const u16*)d_in[9], (const u16*)d_in[10], (char*)d_ws);
  k2_down_conv<<<256, 512, K2_LDS, stream>>>(x, (const u16*)d_in[1], (const u16*)d_in[2], (const u16*)d_in[3],
                                             (const u16*)d_in[4], (const u16*)d_in[5], (const u16*)d_in[6],
                                             (const u16*)d_in[11], (char*)d_ws, (u16*)d_out);
}
```

```cpp
#define HF_WT
#define SVC_PRIO 3
#include <hip/hip_runtime.h>
#include <stdint.h>

typedef unsigned short u16;
typedef __attribute__((ext_vector_type(8))) __bf16 bf16x8;
typedef __attribute__((ext_vector_type(2))) __bf16 bf16x2;
typedef __attribute__((ext_vector_type(8))) _Float16 f16x8;
typedef __attribute__((ext_vector_type(2))) _Float16 f16x2;
typedef __attribute__((ext_vector_type(4))) short s16x4;
typedef __attribute__((ext_vector_type(2))) float f32x2;
typedef __attribute__((ext_vector_type(4))) float f32x4;
typedef __attribute__((ext_vector_type(16))) float f32x16;
typedef __attribute__((ext_vector_type(4))) unsigned u32x4;
typedef __attribute__((ext_vector_type(2))) unsigned u32x2;
typedef __attribute__((ext_vector_type(8))) unsigned u32x8;
typedef __attribute__((ext_vector_type(16))) __bf16 bf16x16;
#define LAS __attribute__((address_space(3)))
#define GAS __attribute__((address_space(1)))
#if defined(__HIP_DEVICE_COMPILE__) && !defined(KEEP_PK)
#define NOPK __attribute__((target("no-packed-fp32-ops")))
#else
#define NOPK
#endif
#define DI __device__ __forceinline__ NOPK

constexpr int L_ = 2048, D_ = 512, N_ = 128, F_ = 1792;

DI float u2f(unsigned v) { return __builtin_bit_cast(float, v); }
DI unsigned f2u(float v) { return __builtin_bit_cast(unsigned, v); }
DI bool wave_any(bool p) { return __builtin_amdgcn_ballot_w64(p) != 0ull; }
DI float bf2f(u16 v) { return u2f(((unsigned)v) << 16); }
DI float bflo(unsigned v) { return u2f(v << 16); }
DI float bfhi(unsigned v) { return u2f(v & 0xffff0000u); }
DI unsigned pk_bf16(float a, float b) { f32x2 v = {a, b}; return __builtin_bit_cast(unsigned, __builtin_convertvector(v, bf16x2)); }
DI unsigned pk_f16(float a, float b) { f32x2 v = {a, b}; return __builtin_bit_cast(unsigned, __builtin_convertvector(v, f16x2)); }
DI unsigned pk_f16v(f32x2 v) { return __builtin_bit_cast(unsigned, __builtin_convertvector(v, f16x2)); }
DI bf16x2 as_bf2(unsigned v) { return __builtin_bit_cast(bf16x2, v); }
DI f16x2 as_h2(unsigned v) { return __builtin_bit_cast(f16x2, v); }
DI bf16x8 as_bf8(u32x4 v) { return __builtin_bit_cast(bf16x8, v); }
DI f16x8 as_h8(u32x4 v) { return __builtin_bit_cast(f16x8, v); }
DI float rbf(float a) { return bflo(pk_bf16(a, a)); }
DI unsigned bfbits(float a) { return pk_bf16(a, a) & 0xffffu; }
DI float both_halves(float v) {
  auto r = __builtin_amdgcn_permlane32_swap(f2u(v), f2u(v), false, false);
  return u2f(r[0]) + u2f(r[1]);
}
DI void lo_hi(float v, float& lo, float& hi) {
  auto r = __builtin_amdgcn_permlane32_swap(f2u(v), f2u(v), false, false);
  lo = u2f(r[0]); hi = u2f(r[1]);
}
DI float wave_absmax(float x) {
  unsigned v = f2u(x);
  v = max(v, (unsigned)__builtin_amdgcn_update_dpp(0, v, 0xB1, 0xf, 0xf, true));
  v = max(v, (unsigned)__builtin_amdgcn_update_dpp(0, v, 0x4E, 0xf, 0xf, true));
  v = max(v, (unsigned)__builtin_amdgcn_update_dpp(0, v, 0x141, 0xf, 0xf, true));
  v = max(v, (unsigned)__builtin_amdgcn_update_dpp(0, v, 0x140, 0xf, 0xf, true));
  unsigned a = __builtin_amdgcn_readlane(v, 0), b = __builtin_amdgcn_readlane(v, 16), c = __builtin_amdgcn_readlane(v, 32),
           d = __builtin_amdgcn_readlane(v, 48);
  return u2f(max(max(a, b), max(c, d)));
}
DI float wave_sum(float x) {
  x += u2f(__builtin_amdgcn_update_dpp(0, f2u(x), 0xB1, 0xf, 0xf, true));
  x += u2f(__builtin_amdgcn_update_dpp(0, f2u(x), 0x4E, 0xf, 0xf, true));
  x += u2f(__builtin_amdgcn_update_dpp(0, f2u(x), 0x141, 0xf, 0xf, true));
  x += u2f(__builtin_amdgcn_update_dpp(0, f2u(x), 0x140, 0xf, 0xf, true));
  const unsigned v = f2u(x);
  return (u2f(__builtin_amdgcn_readlane(v, 0)) + u2f(__builtin_amdgcn_readlane(v, 16))) +
         (u2f(__builtin_amdgcn_readlane(v, 32)) + u2f(__builtin_amdgcn_readlane(v, 48)));
}
DI float next_lane(float v) { return u2f(__builtin_amdgcn_update_dpp(0, f2u(v), 0x130, 0xf, 0xf, true)); }
DI float rdlane(float v, int l) { return u2f(__builtin_amdgcn_readlane(f2u(v), l)); }

#define WAIT_VM(n) asm volatile("s_waitcnt vmcnt(" #n ")" ::: "memory")
#define SCHED() __builtin_amdgcn_sched_barrier(0)
#define WAIT_LGKM0() asm volatile("s_waitcnt lgkmcnt(0)" ::: "memory")
#define BARRIER() do { SCHED(); asm volatile("s_waitcnt lgkmcnt(0)" ::: "memory"); __builtin_amdgcn_s_barrier(); asm volatile("" ::: "memory"); SCHED(); } while (0)

constexpr size_t WS_HF = 0;
constexpr size_t WS_W2F = WS_HF + (size_t)64 * 112 * 1024;
constexpr size_t WS_UT = WS_W2F + (size_t)16 * 112 * 1024;
constexpr size_t WS_ST = WS_UT + (size_t)512 * 4096;
constexpr size_t WS_GR = WS_ST + (size_t)512 * 4096;
constexpr size_t WS_EP = WS_GR + (size_t)2048 * 16;
constexpr size_t WS_KB = WS_EP + 32 * 128;
constexpr size_t WS_SC = WS_KB + (size_t)512 * 4160;
constexpr unsigned WS_END = (unsigned)(WS_SC + 512 * 4);

#define ST_DECL
#define ST()
#define ST_OUT(kid)

DI f32x2 cexp_p(float zr2, float zi, float p) {
  float th = zi * p;
  float n = __builtin_rintf(th * 0.15915494f);
  float rr = fmaf(th, 0.15915494f, -n);
  rr = fmaf(th, 6.4206382e-9f, rr);
  float mag = __builtin_amdgcn_exp2f(zr2 * p);
  return (f32x2){mag * __builtin_amdgcn_cosf(rr), mag * __builtin_amdgcn_sinf(rr)};
}
DI float half_wave_max(float x) {
  unsigned v = f2u(x);
  v = max(v, (unsigned)__builtin_amdgcn_update_dpp(0, v, 0xB1, 0xf, 0xf, true));
  v = max(v, (unsigned)__builtin_amdgcn_update_dpp(0, v, 0x4E, 0xf, 0xf, true));
  v = max(v, (unsigned)__builtin_amdgcn_update_dpp(0, v, 0x141, 0xf, 0xf, true));
  v = max(v, (unsigned)__builtin_amdgcn_update_dpp(0, v, 0x140, 0xf, 0xf, true));
  auto r = __builtin_amdgcn_permlane16_swap(v, v, false, false);
  return u2f(max((unsigned)r[0], (unsigned)r[1]));
}
constexpr int STG = 36864;
constexpr int K1_TAB = 3 * STG;
constexpr int K1_LN = K1_TAB + 2 * 53 * 272;
constexpr int K1_W2 = K1_LN + 8192;
constexpr int K1_MS = K1_W2 + 7168;
constexpr int K1_LDS = K1_MS + 256;

DI void dma_x(unsigned lds, const char* xb, unsigned vx) {
  asm volatile("s_mov_b32 m0, %2\n\ts_nop 0\n\tglobal_load_lds_dwordx4 %0, %1" ::"v"(vx), "s"(xb), "s"(lds) : "memory");
}
DI void dma_w(unsigned lds, const char* wb, const unsigned (&vw)[4]) {
  asm volatile(
      "s_mov_b32 m0, %5\n\ts_nop 0\n\t"
      "global_load_lds_dwordx4 %0, %4\n\t"
      "global_load_lds_dwordx4 %1, %4 offset:1024\n\t"
      "global_load_lds_dwordx4 %2, %4 offset:2048\n\t"
      "global_load_lds_dwordx4 %3, %4 offset:3072" ::"v"(vw[0]),
      "v"(vw[1]), "v"(vw[2]), "v"(vw[3]), "s"(wb), "s"(lds)
      : "memory");
}
DI void dma_w2(unsigned lds, const char* wb, unsigned v0, unsigned v1) {
  asm volatile(
      "s_mov_b32 m0, %3\n\ts_nop 0\n\t"
      "global_load_lds_dwordx4 %0, %2\n\t"
      "global_load_lds_dwordx4 %1, %2 offset:1024" ::"v"(v0), "v"(v1), "s"(wb), "s"(lds)
      : "memory");
}
DI u32x2 tr_rd(const char* p) { return __builtin_bit_cast(u32x2, __builtin_amdgcn_ds_read_tr16_b64_v4i16((LAS s16x4*)p)); }

DI void st_wt(char* p, u32x4 v) {
  asm volatile("global_store_dwordx4 %0, %1, off sc1\n\ts_nop 1" ::"v"(p), "v"(v) : "memory");
}
DI f32x2 cmul(f32x2 a, f32x2 b, f32x2 bn) {
  f32x2 r2 = {a[0], a[0]}, i2 = {a[1], a[1]};
  return __builtin_elementwise_fma(i2, bn, r2 * b);
}
DI f32x2 rot(f32x2 b) { return (f32x2){-b[1], b[0]}; }
DI unsigned mix(unsigned lo, unsigned hi) { return (lo & 0xffffu) | (hi & 0xffff0000u); }
DI unsigned nxt(unsigned v) { return __builtin_amdgcn_update_dpp(0, v, 0x130, 0xf, 0xf, true); }

DI void ln_a(u32x4 xr, u32x2 ep, char* ws, int lane, int token, float& m_, float& rho) {
  float v[8];
#pragma unroll
  for (int i = 0; i < 4; ++i) { v[2 * i] = bflo(xr[i]); v[2 * i + 1] = bfhi(xr[i]); }
  const float mu = wave_sum(((v[0] + v[1]) + (v[2] + v[3])) + ((v[4] + v[5]) + (v[6] + v[7]))) * (1.f / 512.f);
  float q = 0.f;
#pragma unroll
  for (int i = 0; i < 8; ++i) { const float d = v[i] - mu; q = fmaf(d, d, q); }
  const float var = wave_sum(q) * (1.f / 512.f);
  const unsigned mb = bfbits(mu);
  const unsigned sb = bfbits(__builtin_amdgcn_sqrtf(rbf(rbf(var) + 1e-5f)));
  m_ = bflo(mb);
  rho = __builtin_amdgcn_rcpf(bflo(sb));
  const unsigned long long tag = ((((unsigned long long)ep[1] << 32) | ep[0]) + 1ull) << 16;
  const u32x4 gr = {(unsigned)tag | mb, (unsigned)(tag >> 32), (unsigned)tag | sb, (unsigned)(tag >> 32)};
  if (lane == 0) st_wt(ws + WS_GR + (size_t)token * 16, gr);
}
DI void ln_b(u32x4 xr, float m_, float rho, char* smem, int w, int lane) {
  u32x4 o;
#pragma unroll
  for (int i = 0; i < 4; ++i) {
    const unsigned d = pk_bf16(bflo(xr[i]) - m_, bfhi(xr[i]) - m_);
    const unsigned n = pk_bf16(bflo(d) * rho, bfhi(d) * rho);
    o[i] = pk_f16(bflo(n), bfhi(n));
  }
  *(u32x4*)(smem + K1_LN + (7 - w) * 1024 + lane * 16) = o;
}
DI void u_store(char* smem, char* ws, int w, int lane, int tb) {
  const char* p = smem + K1_LN + ((lane & 15) >> 2) * 1024 + (64 * w + (lane >> 4) * 16 + (lane & 3) * 4) * 2;
  const u32x2 a = tr_rd(p), c = tr_rd(p + 4096);
  st_wt(ws + WS_UT + (size_t)tb * 8192 + (64 * w + lane) * 16, (u32x4){a[0], a[1], c[0], c[1]});
}

struct Mode { f32x2 c, e1, w8, G; };
DI Mode mode_prep(float lar, float aim, float cr, float ci, float dt) {
  const float zr2 = -rbf(expf(lar)) * dt * 1.44269504f, zi = aim * dt;
  Mode m;
  m.c = (f32x2){cr, -ci};
  m.e1 = cexp_p(zr2, -zi, 1.f);
  m.w8 = cexp_p(zr2, zi, 8.f);
  m.G = cexp_p(zr2, -zi, 256.f);
  return m;
}
#define PUT(row, v) *(unsigned*)(tab + (row) * 272) = pk_f16v(v)
struct Chain { f32x2 e1n, Gn, e2, e2n, e4, e4n, w16, w16n, R, Gq, T, Ee, Eo; };
DI void tab_part(char* tab, const Mode& m, Chain& c, const int k0, const int k1) {
  if (k0 == 0) {
    c.e1n = rot(m.e1); c.Gn = rot(m.G);
    c.w16 = cmul(m.w8, m.w8, rot(m.w8)); c.w16n = rot(c.w16);
    c.e2 = cmul(m.e1, m.e1, c.e1n); c.e2n = rot(c.e2); c.e4 = cmul(c.e2, c.e2, c.e2n); c.e4n = rot(c.e4);
    c.R = m.c; c.Gq = cmul(m.c, m.G, c.Gn); c.T = c.Gq; c.Ee = c.w16; c.Eo = m.w8;
    PUT(0, c.R); PUT(8, c.Gq); *(unsigned*)(tab + 21 * 272) = 0x00003C00u; PUT(22, c.Eo); PUT(23, c.Ee);
  }
#pragma unroll
  for (int k = (k0 ? k0 : 1); k < k1; ++k) {
    c.Eo = cmul(c.Eo, c.w16, c.w16n); PUT(22 + 2 * k, c.Eo);
    if (k < 15) { c.Ee = cmul(c.Ee, c.w16, c.w16n); PUT(23 + 2 * k, c.Ee); }
    const int j = (k + 1) >> 1;
    if ((k & 1) && j < 8) { c.R = cmul(c.R, m.e1, c.e1n); PUT(j, c.R); }
    if (!(k & 1)) {
      const int g = k >> 1;
      c.Gq = cmul(c.Gq, m.G, c.Gn);
      if (g == 1) { PUT(12, c.Gq); f32x2 q = cmul(c.Gq, c.e4, c.e4n); PUT(13, q); }
      else if (g == 2) { PUT(14, c.Gq); f32x2 q = cmul(c.Gq, c.e4, c.e4n); PUT(15, q); }
      else PUT(13 + g, c.Gq);
    }
    if (k >= 5 && k < 8) { c.T = cmul(c.T, c.e2, c.e2n); PUT(8 + k - 4, c.T); }
  }
}
DI void tab_mma(const char* tb, f32x16& ka) {
  u32x4 ta[8], tc[8];
#pragma unroll
  for (int k = 0; k < 8; ++k) { ta[k] = *(const u32x4*)(tb + k * 32); tc[k] = *(const u32x4*)(tb + 21 * 272 + k * 32); }
#pragma unroll
  for (int k = 0; k < 8; ++k) ka = __builtin_amdgcn_mfma_f32_32x32x16_f16(as_h8(ta[k]), as_h8(tc[k]), ka, 0, 0, 0);
}
DI void k_expand(const f32x16& ka, char* kb, int lane) {
  const int r = lane & 31, h = lane >> 5;
  unsigned D[12];
#pragma unroll
  for (int i = 0; i < 12; ++i) D[i] = pk_f16(ka[i], ka[i]);
  char* c0 = kb + 32;
  const bool last = (r == 31);
  const unsigned f512 = __builtin_amdgcn_readlane(D[4], 32), f768 = __builtin_amdgcn_readlane(D[6], 32),
                 f1024 = __builtin_amdgcn_readlane(D[8], 0), f1280 = __builtin_amdgcn_readlane(D[9], 0),
                 f1536 = __builtin_amdgcn_readlane(D[10], 0), f1792 = __builtin_amdgcn_readlane(D[11], 0),
                 f2048 = __builtin_amdgcn_readlane(D[8], 32);
  *(u32x2*)(c0 + (8 * r + 4 * h) * 2) = (u32x2){mix(D[0], D[1]), mix(D[2], D[3])};
  {
    const unsigned n4 = nxt(D[4]);
    const unsigned nn = last ? (h ? f768 : f512) : n4;
    const unsigned e1_ = h ? D[4] : D[5], e2_ = h ? D[5] : D[6], e3_ = h ? nn : D[7];
    *(u32x4*)(c0 + (256 + 256 * h + 8 * r) * 2) = (u32x4){D[4], mix(e1_, e2_), e2_, mix(e3_, nn)};
  }
  {
    const unsigned k0 = h ? D[6] : D[8];
    const unsigned n0 = nxt(k0);
    const unsigned nn = last ? (h ? f1024 : f1280) : n0;
    const unsigned e1_ = h ? D[7] : k0, e2_ = h ? D[7] : ((r & 1) ? nn : k0), e3_ = h ? D[7] : nn;
    *(u32x4*)(c0 + (h ? 768 + 8 * r : 1024 + 8 * r) * 2) = (u32x4){k0, mix(k0, e1_), mix(e2_, e3_), nn};
  }
  const unsigned fl[3] = {f1536, f1792, f2048};
#pragma unroll
  for (int a = 0; a < 3; ++a) {
    const unsigned k0 = D[9 + a], n0 = nxt(k0), nn = last ? fl[a] : n0;
    if (h == 0) *(u32x4*)(c0 + (1280 + 256 * a + 8 * r) * 2) = (u32x4){k0, k0, (r & 1) ? nn : mix(k0, nn), nn};
  }
  if (lane >= 60) *(u32x4*)(kb + ((lane & 2) ? 4128 - 32 : 0) + (lane & 3) * 16) = (u32x4){0u, 0u, 0u, 0u};
}

__global__ __launch_bounds__(576, 3) NOPK void k1_ffn_up(const u16* __restrict__ x, const u16* __restrict__ w1,
                                                   const u16* __restrict__ b1, const u16* __restrict__ w2,
                                                   const u16* __restrict__ lar, const u16* __restrict__ aim,
                                                   const u16* __restrict__ cre, const u16* __restrict__ cim,
                                                   const u16* __restrict__ ldt, char* __restrict__ ws) {
  extern __shared__ __attribute__((aligned(16))) char smem[];
  const int tid = threadIdx.x, lane = tid & 63;
  const int w = __builtin_amdgcn_readfirstlane(tid >> 6);
  const int b = blockIdx.x, xcd = b & 7, jj = b >> 3;
  const int rb = xcd * 4 + (jj & 3), cb = jj >> 2;
  const int r = lane & 31, h = lane >> 5;
  const unsigned lds0 = (unsigned)(size_t)smem;
  ST_DECL; ST();
  const char* xb = (const char*)x;
  const char* wb = (const char*)w1;
  const int tb = rb * 8 + cb;
#define LOAD_ROW() \
  u32x4 xr; u32x2 ep; \
  { const unsigned vo = (unsigned)((tb * 8 + w) * 1024 + lane * 16); const char* epp = ws + WS_EP + rb * 128; const unsigned z = 0; \
    asm volatile("global_load_dwordx4 %0, %2, %3\n\tglobal_load_dwordx2 %1, %4, %5 sc1" \
                 : "=&v"(xr), "=&v"(ep) : "v"(vo), "s"(xb), "v"(z), "s"(epp) : "memory"); }
  unsigned vx;
  {
    const int tk = w * 8 + (lane >> 3);
    vx = (unsigned)((rb * 64 + tk) * 1024 + (((lane & 7) ^ ((tk >> 1) & 7)) << 4));
  }
  const unsigned ldx = lds0 + w * 1024;
  const int blk = (lane >> 4) & 1, q4 = (lane & 15) >> 2, p4 = lane & 3;
  float m_, rho_;

  if (w < 7) {
    dma_x(ldx, xb, vx);
    unsigned vw[4];
#pragma unroll
    for (int m = 0; m < 4; ++m) {
      int c = (4 * w + m) * 64 + lane, row = c / 28, cc = c - row * 28;
      vw[m] = (unsigned)(row * 3584 + cb * 448 + cc * 16 - m * 1024);
    }
    const unsigned ldw = lds0 + 8192 + w * 4096;
    dma_w(ldw, wb, vw);
    LOAD_ROW();
    ST();
    BARRIER();
    dma_x(ldx + STG, xb + 128, vx);
    dma_w(ldw + STG, wb + (size_t)64 * 3584, vw);
    const unsigned aoff = 8192 + (8 * h + q4) * 448 + (w * 32 + 16 * blk + 4 * p4) * 2;
    unsigned boff[4];
#pragma unroll
    for (int k = 0; k < 4; ++k) boff[k] = r * 128 + (((2 * k + h) ^ ((r >> 1) & 7)) << 4);
    f32x16 acc[2];
#pragma unroll
    for (int i = 0; i < 16; ++i) { acc[0][i] = 0.f; acc[1][i] = 0.f; }
    const int row16 = lane & 15;
    const unsigned one2 = 0x3F803F80u;
    const u32x4 sa = {row16 < 2 ? one2 : 0u, row16 < 2 ? one2 : 0u, (row16 & 14) == 2 ? one2 : 0u, (row16 & 14) == 2 ? one2 : 0u};
    const int sidx = (lane & 1) ? 0xEEEEEEEE : 0x44444444;
    f32x4 csa = {0.f, 0.f, 0.f, 0.f};
    u32x8 A[2][2];
    u32x4 B0[2][4], B1[2][4];
    u16 b1v;
    unsigned ms[2];
    ST();
#pragma unroll
    for (int s = 0; s <= 8; ++s) {
      if (s == 0) WAIT_VM(7);
      else if (s == 1) asm volatile("s_waitcnt vmcnt(5)" : "+v"(xr), "+v"(ep)::"memory");
      else if (s <= 5) WAIT_VM(6); else if (s == 6) WAIT_VM(5); else if (s == 7) WAIT_VM(0);
      if (s < 8) BARRIER();
      if (s == 0 || s == 1 || s == 4 || s == 7) ST();
      const char* sb = smem + (s % 3) * STG;
      const int p = (s - 1) & 1;
      const bool dm = (s + 2 <= 7);
#pragma unroll
      for (int k = 0; k < 4; ++k) {
        const u32x8 a8 = A[p][k >> 1];
        const u32x4 a4 = (k & 1) ? (u32x4){a8[4], a8[5], a8[6], a8[7]} : (u32x4){a8[0], a8[1], a8[2], a8[3]};
        if (s >= 1) acc[0] = __builtin_amdgcn_mfma_f32_32x32x16_bf16(as_bf8(a4), as_bf8(B0[p][k]), acc[0], 0, 0, 0);
        SCHED();
        if (s < 8) {
          const u32x2 al = tr_rd(sb + aoff + (16 * k) * 448), ah = tr_rd(sb + aoff + (16 * k + 4) * 448);
          A[s & 1][k >> 1][4 * (k & 1)] = al[0]; A[s & 1][k >> 1][4 * (k & 1) + 1] = al[1];
          A[s & 1][k >> 1][4 * (k & 1) + 2] = ah[0]; A[s & 1][k >> 1][4 * (k & 1) + 3] = ah[1];
          B0[s & 1][k] = *(const u32x4*)(sb + boff[k]);
          B1[s & 1][k] = *(const u32x4*)(sb + 4096 + boff[k]);
        }
        SCHED();
        if (s >= 1) acc[1] = __builtin_amdgcn_mfma_f32_32x32x16_bf16(as_bf8(a4), as_bf8(B1[p][k]), acc[1], 0, 0, 0);
        if (s >= 1 && (k & 1)) csa = __builtin_amdgcn_smfmac_f32_16x16x64_bf16(as_bf8(sa), __builtin_bit_cast(bf16x16, a8), csa, sidx, 0, 0);
        SCHED();
        if (dm) {
          if (k == 0) dma_x(ldx + ((s + 2) % 3) * STG, xb + (s + 2) * 128, vx);
          if (k == 1) dma_w2(ldw + ((s + 2) % 3) * STG, wb + (size_t)(s + 2) * 64 * 3584, vw[0], vw[1]);
          if (k == 2) dma_w2(ldw + ((s + 2) % 3) * STG + 2048, wb + (size_t)(s + 2) * 64 * 3584 + 2048, vw[2], vw[3]);
        }
        SCHED();
      }
      if (s == 1) ln_a(xr, ep, ws, lane, tb * 8 + w, m_, rho_);
      if (s == 2) ln_b(xr, m_, rho_, smem, w, lane);
      if (s == 3) u_store(smem, ws, w, lane, tb);
      if (s == 7) {
        b1v = b1[cb * 224 + w * 32 + r];
        ms[0] = *(const unsigned*)(smem + K1_MS + r * 4);
        ms[1] = *(const unsigned*)(smem + K1_MS + 128 + r * 4);
      }
      SCHED();
    }
    const float cs = u2f(__builtin_amdgcn_permlane16_swap(f2u(csa[0] + csa[1]), f2u(csa[2] + csa[3]), false, false)[0]);
    const float hi = rbf(cs), r1 = cs - hi, mid = rbf(r1), lo = r1 - mid;
    u32x4 a = {pk_bf16(hi, mid), bfbits(lo) | ((unsigned)b1v << 16), 0u, 0u};
    if (h) { a[0] = 0u; a[1] = 0u; }
#pragma unroll
    for (int t = 0; t < 2; ++t) {
      const unsigned nm = (ms[t] & 0xffffu) ^ 0x8000u;
      const u32x4 bb = {nm | (nm << 16), nm | (ms[t] & 0xffff0000u), 0u, 0u};
      acc[t] = __builtin_amdgcn_mfma_f32_32x32x16_bf16(as_bf8(a), as_bf8(bb), acc[t], 0, 0, 0);
    }
    ST();
#pragma unroll
    for (int t = 0; t < 2; ++t) {
      const float sdv = bfhi(ms[t]), rho = __builtin_amdgcn_rcpf(sdv);
      const float ca = -2.302208198f * rho, cbb = -0.1029432397f * rho * rho * rho;
      char* hp = ws + WS_HF + ((size_t)((rb * 2 + t) * 112 + cb * 14 + 2 * w) * 64 + lane) * 16;
#pragma unroll
      for (int hh = 0; hh < 2; ++hh) {
        unsigned o[4];
#pragma unroll
        for (int i = 0; i < 4; ++i) {
          float g[2];
#pragma unroll
          for (int e = 0; e < 2; ++e) {
            float d = acc[t][8 * hh + 2 * i + e];
            float ex = __builtin_amdgcn_exp2f(d * fmaf(d * d, cbb, ca));
            g[e] = d * __builtin_amdgcn_rcpf(fmaf(ex, sdv, sdv));
          }
          o[i] = pk_bf16(g[0], g[1]);
        }
        st_wt(hp + hh * 1024, (u32x4){o[0], o[1], o[2], o[3]});
      }
    }
  } else {
    const int ch = 2 * (xcd * 32 + jj) + (w - 7);
#define LOAD_PAR() \
    unsigned q_lar, q_aim, q_cre, q_cim, q_ldt; \
    { const unsigned vo = ch * 256 + lane * 4, vd = ch * 2; \
      asm volatile("global_load_dword %0, %5, %7\n\tglobal_load_dword %1, %5, %8\n\tglobal_load_dword %2, %5, %9\n\t" \
                   "global_load_dword %3, %5, %10\n\tglobal_load_ushort %4, %6, %11" \
                   : "=&v"(q_lar), "=&v"(q_aim), "=&v"(q_cre), "=&v"(q_cim), "=&v"(q_ldt) \
                   : "v"(vo), "v"(vd), "s"(lar), "s"(aim), "s"(cre), "s"(cim), "s"(ldt) : "memory"); }
#define COMMON() \
    const float dt = rbf(expf(bflo(q_ldt))); \
    const float cr0 = bflo(q_cre) * dt, cr1 = bfhi(q_cre) * dt, ci0 = bflo(q_cim) * dt, ci1 = bfhi(q_cim) * dt; \
    const int ce = max(__builtin_amdgcn_frexp_expf(wave_absmax(fmaxf(fmaxf(fabsf(cr0), fabsf(cr1)), fmaxf(fabsf(ci0), fabsf(ci1))))), -100); \
    const float csc = __builtin_amdgcn_ldexpf(1.f, -ce);
    char* tab = smem + K1_TAB + (w - 7) * (53 * 272) + lane * 4;
    const char* tbr = smem + K1_TAB + (w - 7) * (53 * 272) + r * 272 + h * 16;
    f32x16 ka;
#pragma unroll
    for (int i = 0; i < 16; ++i) ka[i] = 0.f;
    Chain cn;
    __builtin_amdgcn_s_setprio(SVC_PRIO);
    if (w == 7) {
      dma_x(ldx, xb, vx);
      LOAD_ROW();
      LOAD_PAR();
      ST();
      BARRIER();
      dma_x(ldx + STG, xb + 128, vx);
      ST();
#define XDMA(s) dma_x(ldx + ((s) % 3) * STG, xb + (s) * 128, vx)
      WAIT_VM(8); BARRIER(); ST();
      XDMA(2);
      asm volatile("s_waitcnt vmcnt(1)" : "+v"(xr), "+v"(ep), "+v"(q_lar), "+v"(q_aim), "+v"(q_cre), "+v"(q_cim), "+v"(q_ldt)::"memory");
      BARRIER(); ST();
      XDMA(3);
      ln_a(xr, ep, ws, lane, tb * 8 + w, m_, rho_);
      COMMON();
      WAIT_VM(2); BARRIER();
      XDMA(4);
      ln_b(xr, m_, rho_, smem, w, lane);
      const Mode m0 = mode_prep(bflo(q_lar), bflo(q_aim), cr0 * csc, ci0 * csc, dt);
      WAIT_VM(2); BARRIER();
      XDMA(5);
      u_store(smem, ws, w, lane, tb);
      tab_part(tab, m0, cn, 0, 7);
      WAIT_VM(2); BARRIER(); ST();
      XDMA(6);
      tab_part(tab, m0, cn, 7, 16);
      WAIT_VM(2); BARRIER();
      XDMA(7);
      const unsigned goff = (unsigned)WS_GR + (rb * 64 + lane) * 16;
      u32x4 gr;
      asm volatile("global_load_dwordx4 %0, %1, %2 sc1" : "=&v"(gr) : "v"(goff), "s"(ws) : "memory");
      tab_mma(tbr, ka);
      const Mode m1 = mode_prep(bfhi(q_lar), bfhi(q_aim), cr1 * csc, ci1 * csc, dt);
      WAIT_VM(2); BARRIER();
      tab_part(tab, m1, cn, 0, 7);
      const unsigned long long e1 = (((unsigned long long)ep[1] << 32) | ep[0]) + 1ull;
      const unsigned tl = (unsigned)(e1 << 16), th = (unsigned)(e1 >> 16);
      asm volatile("s_waitcnt vmcnt(0)" : "+v"(gr)::"memory");
      for (unsigned spins = 0; spins < (1u << 20); ++spins) {
        const bool ok = (gr[0] & 0xffff0000u) == tl && gr[1] == th && (gr[2] & 0xffff0000u) == tl && gr[3] == th;
        if (!wave_any(!ok)) break;
        __builtin_amdgcn_s_sleep(2);
        asm volatile("global_load_dwordx4 %0, %1, %2 sc1\n\ts_waitcnt vmcnt(0)" : "=&v"(gr) : "v"(goff), "s"(ws) : "memory");
      }
      *(unsigned*)(smem + K1_MS + lane * 4) = (gr[0] & 0xffffu) | (gr[2] << 16);
      BARRIER(); ST();
      if (cb == 0 && lane == 0)
        __hip_atomic_store((GAS unsigned long long*)(ws + WS_EP + rb * 128), e1, __ATOMIC_RELAXED, __HIP_MEMORY_SCOPE_AGENT);
      tab_part(tab, m1, cn, 7, 16);
      tab_mma(tbr, ka);
      if (lane == 0) *(float*)(ws + WS_SC + ch * 4) = __builtin_amdgcn_ldexpf(1.f, ce + 1);
    } else {
      LOAD_PAR();
      ST();
      BARRIER();
      {
        const unsigned vo = (lane >> 2) * 1024 + (lane & 3) * 16;
#pragma unroll
        for (int f = 0; f < 7; ++f) {
          const int gw = b * 7 + f, dt = gw / 112, ks = gw - dt * 112;
          dma_x(lds0 + K1_W2 + f * 1024, (const char*)w2 + ks * 16384 + dt * 64, vo);
        }
      }
      ST();
      BARRIER(); ST();
      asm volatile("s_waitcnt vmcnt(7)" : "+v"(q_lar), "+v"(q_aim), "+v"(q_cre), "+v"(q_cim), "+v"(q_ldt)::"memory");
      COMMON();
      BARRIER(); ST();
      const Mode m0 = mode_prep(bflo(q_lar), bflo(q_aim), cr0 * csc, ci0 * csc, dt);
      {
        const u32x4 sent = {~0u, ~0u, ~0u, ~0u};
        char* sp = ws + WS_ST + (size_t)b * 8192 + lane * 16;
#pragma unroll
        for (int i = 0; i < 8; ++i) st_wt(sp + i * 1024, sent);
      }
      BARRIER();
      tab_part(tab, m0, cn, 0, 7);
      BARRIER();
      tab_part(tab, m0, cn, 7, 16);
      BARRIER(); ST();
      tab_mma(tbr, ka);
      const Mode m1 = mode_prep(bfhi(q_lar), bfhi(q_aim), cr1 * csc, ci1 * csc, dt);
      WAIT_VM(8);
      {
        const char* p = smem + K1_W2 + (4 * h + q4) * 64 + (16 * blk + 4 * p4) * 2;
#pragma unroll
        for (int f = 0; f < 7; ++f) {
          const u32x2 a = tr_rd(p + f * 1024), c = tr_rd(p + f * 1024 + 512);
          st_wt(ws + WS_W2F + (size_t)(b * 7 + f) * 1024 + lane * 16, (u32x4){a[0], a[1], c[0], c[1]});
        }
      }
      BARRIER();
      tab_part(tab, m1, cn, 0, 7);
      BARRIER();
      tab_part(tab, m1, cn, 7, 16);
      BARRIER(); ST();
      tab_mma(tbr, ka);
      if (lane == 0) *(float*)(ws + WS_SC + ch * 4) = __builtin_amdgcn_ldexpf(1.f, ce + 1);
    }
    ST();
    k_expand(ka, ws + WS_KB + (size_t)ch * 4160, lane);
  }
  ST();
}

#define S_GO 20
#define CONV_PRIO 3
constexpr int CP_STRIDE = 4352 + 64;
constexpr int CV_CP = 0;
constexpr int CV_U = 4 * CP_STRIDE;
constexpr int CV_XY = CV_U + 4608;
constexpr int CV_BYTES = CV_XY + 8192;
constexpr int K2_CV = 65536;
constexpr int K2_S = K2_CV + 2 * CV_BYTES;
constexpr int K2_FLAG = K2_S + 64 * 144;
constexpr int K2_LDS = K2_FLAG + 64;
constexpr int K2_S_ROW = 144;

DI void lds_set(LAS unsigned* p, unsigned v) {
  WAIT_LGKM0();
  __hip_atomic_store(p, v, __ATOMIC_RELAXED, __HIP_MEMORY_SCOPE_WORKGROUP);
  asm volatile("" ::: "memory");
}
DI void lds_wait(LAS unsigned* p, unsigned v) {
  while ((unsigned)__builtin_amdgcn_readfirstlane(__hip_atomic_load(p, __ATOMIC_RELAXED, __HIP_MEMORY_SCOPE_WORKGROUP)) < v)
    __builtin_amdgcn_s_sleep(0);
  asm volatile("" ::: "memory");
}

__global__ __launch_bounds__(512, 2) NOPK void k2_down_conv(const u16* __restrict__ x, const u16* __restrict__ dskip,
                                                      const u16* __restrict__ b2, char* __restrict__ ws,
                                                      u16* __restrict__ out) {
  extern __shared__ __attribute__((aligned(16))) char smem[];
  const int tid = threadIdx.x, lane = tid & 63;
  const int w = __builtin_amdgcn_readfirstlane(tid >> 6);
  const int b = blockIdx.x, xcd = b & 7, jj = b >> 3;
  const int rb = xcd * 4 + (jj & 3), cb = jj >> 2;
  const int r = lane & 31, h = lane >> 5;
  LAS unsigned* flags = (LAS unsigned*)(smem + K2_FLAG);
  if (tid < 16) flags[tid] = 0u;
  ST_DECL; ST();
  const auto rs = __builtin_amdgcn_make_buffer_rsrc((void*)ws, 0, WS_END, 0x00020000);

  u16 xv[8];
  u16 b2v;

  if (w < 4) {
    BARRIER();
    const char* ha = ws + WS_HF + ((size_t)((rb * 2) * 112 + 28 * w) * 64 + lane) * 16;
    const char* wbp = ws + WS_W2F + ((size_t)((cb * 2) * 112 + 28 * w) * 64 + lane) * 16;
#define PF 8
    constexpr int P = PF;
    u32x4 fa[P][2], fb[P][2];
#pragma unroll
    for (int k = 0; k < P; ++k) {
      fa[k][0] = *(const u32x4*)(ha + k * 1024);
      fb[k][0] = *(const u32x4*)(wbp + k * 1024);
      fa[k][1] = *(const u32x4*)(ha + 112 * 1024 + k * 1024);
      fb[k][1] = *(const u32x4*)(wbp + 112 * 1024 + k * 1024);
      SCHED();
    }
    ST();
    f32x16 acc[2][2];
#pragma unroll
    for (int i = 0; i < 16; ++i) { acc[0][0][i] = 0.f; acc[0][1][i] = 0.f; acc[1][0][i] = 0.f; acc[1][1][i] = 0.f; }
#pragma unroll
    for (int k = 0; k < 28; ++k) {
      const int sl = k % P;
      u32x4 a0 = fa[sl][0], a1 = fa[sl][1], b0 = fb[sl][0], b1 = fb[sl][1];
      acc[0][0] = __builtin_amdgcn_mfma_f32_32x32x16_bf16(as_bf8(a0), as_bf8(b0), acc[0][0], 0, 0, 0);
      acc[0][1] = __builtin_amdgcn_mfma_f32_32x32x16_bf16(as_bf8(a0), as_bf8(b1), acc[0][1], 0, 0, 0);
      acc[1][0] = __builtin_amdgcn_mfma_f32_32x32x16_bf16(as_bf8(a1), as_bf8(b0), acc[1][0], 0, 0, 0);
      acc[1][1] = __builtin_amdgcn_mfma_f32_32x32x16_bf16(as_bf8(a1), as_bf8(b1), acc[1][1], 0, 0, 0);
      SCHED();
      if (k == 0 || k == 8 || k == 16) ST();
      if (k + P < 28) {
        fa[sl][0] = *(const u32x4*)(ha + (k + P) * 1024);
        fb[sl][0] = *(const u32x4*)(wbp + (k + P) * 1024);
        fa[sl][1] = *(const u32x4*)(ha + 112 * 1024 + (k + P) * 1024);
        fb[sl][1] = *(const u32x4*)(wbp + 112 * 1024 + (k + P) * 1024);
      }
      if (k + P == 28) {
#pragma unroll
        for (int j = 0; j < 8; ++j) xv[j] = x[(size_t)(rb * 64 + w * 8 + j) * D_ + cb * 64 + lane];
        b2v = b2[cb * 64 + lane];
      }
      if (k == S_GO) __hip_atomic_store(flags + 8, 1u, __ATOMIC_RELAXED, __HIP_MEMORY_SCOPE_WORKGROUP);
      SCHED();
    }
    ST();
#pragma unroll
    for (int a = 0; a < 2; ++a)
#pragma unroll
      for (int bb = 0; bb < 2; ++bb)
#pragma unroll
        for (int g = 0; g < 4; ++g) {
          const int d = 32 * bb + r, ch = 8 * a + 2 * g + h;
          f32x4 v = {acc[a][bb][4 * g], acc[a][bb][4 * g + 1], acc[a][bb][4 * g + 2], acc[a][bb][4 * g + 3]};
          *(f32x4*)(smem + w * 16384 + d * 256 + ((ch ^ (d & 15)) << 4)) = v;
        }
  } else {
    const int cw = w & 3, chl = cw >> 1, hf = cw & 1;
    const int c = 2 * (xcd * 32 + jj) + chl;
    char* cv = smem + K2_CV + chl * CV_BYTES;
    u32x4 lo[3];
    u32x2 hi[3];
    int chs[3];
    const char* kb = ws + WS_KB + (size_t)c * 4160;
#pragma unroll
    for (int rd = 0; rd < 3; ++rd) {
      const int idx = min(rd * 64 + lane, 143);
      const int k1 = idx / 18;
      chs[rd] = (16 * hf + (idx - 18 * k1) + 32 * k1) * 16;
      lo[rd] = *(const u32x4*)(kb + chs[rd]);
      hi[rd] = *(const u32x2*)(kb + chs[rd] + 16);
    }
    const char* ug = ws + WS_UT + (size_t)(255 - 128 * hf - lane) * 8192 + c * 16;
    const u32x4 u0 = *(const u32x4*)ug, u1 = *(const u32x4*)(ug - 64 * 8192);
    const float kun = *(const float*)(ws + WS_SC + c * 4);
    const u16 p_ds = dskip[c];
    __builtin_amdgcn_s_setprio(CONV_PRIO);
    SCHED();
    BARRIER();
    if (lane < 16) *(u32x4*)(cv + CV_U + 4096 + hf * 256 + lane * 16) = (u32x4){0u, 0u, 0u, 0u};
#pragma unroll
    for (int rd = 0; rd < 3; ++rd) {
      const unsigned d[6] = {lo[rd][0], lo[rd][1], lo[rd][2], lo[rd][3], hi[rd][0], hi[rd][1]};
      unsigned al[5];
#pragma unroll
      for (int i = 0; i < 5; ++i) al[i] = __builtin_amdgcn_alignbit(d[i + 1], d[i], 16);
      char* dst = cv + CV_CP + chs[rd];
      *(u32x4*)dst = lo[rd];
      *(u32x4*)(dst + CP_STRIDE) = (u32x4){al[0], al[1], al[2], al[3]};
      *(u32x4*)(dst + 2 * CP_STRIDE) = (u32x4){d[1], d[2], d[3], d[4]};
      *(u32x4*)(dst + 3 * CP_STRIDE) = (u32x4){al[1], al[2], al[3], al[4]};
    }
    ST();
    *(u32x4*)(cv + CV_U + hf * 2048 + lane * 16) = u0;
    *(u32x4*)(cv + CV_U + hf * 2048 + 1024 + lane * 16) = u1;
    lds_set(flags + cw, 1u);
    lds_wait(flags + (cw ^ 1), 1u);
    ST();
    const int i16 = lane & 15, g4 = lane >> 4, a1 = i16 + 1;
    unsigned apa = (unsigned)(size_t)(cv + CV_CP + (a1 & 3) * CP_STRIDE + (16 * hf + g4) * 16 + (a1 >> 2) * 8), apb = apa + 8;
    asm volatile("" : "+v"(apa), "+v"(apb));
    const LAS char* ap = (const LAS char*)(size_t)apa;
    const LAS char* ap2 = (const LAS char*)(size_t)apb;
    const char* bp = cv + CV_U + (2032 + 128 * hf - 16 * i16 + 8 * g4) * 2;
    f32x4 ya[8];
#pragma unroll
    for (int t = 0; t < 8; ++t) ya[t] = (f32x4){0.f, 0.f, 0.f, 0.f};
    {
      u32x4 A[2][8], B[2][8];
#pragma unroll
      for (int k0 = 0; k0 <= 4; ++k0) {
        if (k0 < 4) {
#pragma unroll
          for (int i = 0; i < 8; ++i) {
            const u32x2 q0 = *(const volatile LAS u32x2*)(ap + k0 * 64 + i * 512), q1 = *(const volatile LAS u32x2*)(ap + k0 * 64 + i * 512 + 8);
            A[k0 & 1][i] = (u32x4){q0[0], q0[1], q1[0], q1[1]};
            B[k0 & 1][i] = *(const u32x4*)(bp + k0 * 64 - i * 512);
          }
          SCHED();
        }
        if (k0 >= 1) {
          const int p = (k0 - 1) & 1;
#pragma unroll
          for (int dl = 0; dl < 8; ++dl)
#pragma unroll
            for (int k1 = 0; k1 + dl < 8; ++k1)
              ya[k1 + dl] = __builtin_amdgcn_mfma_f32_16x16x32_f16(as_h8(A[p][k1]), as_h8(B[p][dl]), ya[k1 + dl], 0, 0, 0);
          SCHED();
        }
      }
    }
    ST();
    {
      char* xy = cv + CV_XY + hf * 4096 + lane * 16;
#pragma unroll
      for (int t = 0; t < 8; ++t) asm volatile("" : "+v"(ya[t]));
      f32x4 mine[4];
      if (hf) {
#pragma unroll
        for (int t = 0; t < 4; ++t) { *(f32x4*)(xy + t * 1024) = ya[t]; mine[t] = ya[4 + t]; }
      } else {
#pragma unroll
        for (int t = 0; t < 4; ++t) { *(f32x4*)(xy + t * 1024) = ya[4 + t]; mine[t] = ya[t]; }
      }
      lds_set(flags + 4 + cw, 1u);
      const float dsk = bf2f(p_ds);
      const int tb0 = 1024 * hf + 16 * i16 + 4 * g4;
      unsigned xd[4][2];
#pragma unroll
      for (int t = 0; t < 4; ++t) {
        const u32x2 uu = *(const u32x2*)(cv + CV_U + (2044 - tb0 - 256 * t) * 2);
        const f16x2 ua = as_h2(uu[0]), ub = as_h2(uu[1]);
        xd[t][0] = pk_bf16((float)ub[1] * dsk, (float)ub[0] * dsk);
        xd[t][1] = pk_bf16((float)ua[1] * dsk, (float)ua[0] * dsk);
      }
      lds_wait(flags + 4 + (cw ^ 1), 1u);
      const char* xo = cv + CV_XY + (hf ^ 1) * 4096 + lane * 16;
      const unsigned so = (unsigned)WS_ST + c * 4096 + tb0 * 2;
#pragma unroll
      for (int t = 0; t < 4; ++t) {
        const f32x4 y = (*(const f32x4*)(xo + t * 1024) + mine[t]) * kun;
        const unsigned y0 = pk_bf16(y[0], y[1]), y1 = pk_bf16(y[2], y[3]);
        unsigned s0 = pk_bf16(bflo(y0) + bflo(xd[t][0]), bfhi(y0) + bfhi(xd[t][0]));
        const unsigned s1 = pk_bf16(bflo(y1) + bflo(xd[t][1]), bfhi(y1) + bfhi(xd[t][1]));
        if ((s0 & 0xffffu) == 0xffffu) s0 ^= 0x3fu;
        __builtin_amdgcn_raw_buffer_store_b64((u32x2){s0, s1}, rs, so + 512 * t, 0, 16);
      }
    }
    __builtin_amdgcn_s_setprio(0);
    ST();
#pragma unroll
    for (int j = 0; j < 8; ++j) xv[j] = x[(size_t)(rb * 64 + w * 8 + j) * D_ + cb * 64 + lane];
    b2v = b2[cb * 64 + lane];
    while ((unsigned)__builtin_amdgcn_readfirstlane(__hip_atomic_load(flags + 8, __ATOMIC_RELAXED, __HIP_MEMORY_SCOPE_WORKGROUP)) == 0u)
      __builtin_amdgcn_s_sleep(4);
    const int drow = 16 * cw + (lane >> 3);
    const unsigned s_off = (unsigned)WS_ST + (cb * 64 + drow) * 4096 + rb * 128 + (lane & 7) * 16;
    u32x4 s0, s1;
    for (unsigned spins = 0; spins < (1u << 20); ++spins) {
      asm volatile("global_load_dwordx4 %0, %2, %4 sc1\n\tglobal_load_dwordx4 %1, %3, %4 sc1\n\ts_waitcnt vmcnt(0)"
                   : "=&v"(s0), "=&v"(s1) : "v"(s_off), "v"(s_off + 8 * 4096), "s"(ws) : "memory");
      const bool bad = ((s0[0] & 0xffffu) == 0xffffu) || ((s0[2] & 0xffffu) == 0xffffu) || ((s1[0] & 0xffffu) == 0xffffu) ||
                       ((s1[2] & 0xffffu) == 0xffffu);
      if (!wave_any(bad)) break;
      __builtin_amdgcn_s_sleep(4);
    }
    *(u32x4*)(smem + K2_S + drow * K2_S_ROW + (lane & 7) * 16) = s0;
    *(u32x4*)(smem + K2_S + (drow + 8) * K2_S_ROW + (lane & 7) * 16) = s1;
  }
  ST();
  BARRIER();
  ST();
  {
    f32x4 v0 = {0.f, 0.f, 0.f, 0.f}, v1 = {0.f, 0.f, 0.f, 0.f};
#pragma unroll
    for (int q = 0; q < 4; ++q) {
      v0 += *(const f32x4*)(smem + q * 16384 + lane * 256 + (((2 * w) ^ (lane & 15)) << 4));
      v1 += *(const f32x4*)(smem + q * 16384 + lane * 256 + (((2 * w + 1) ^ (lane & 15)) << 4));
    }
    const u32x4 sv = *(const u32x4*)(smem + K2_S + lane * K2_S_ROW + w * 16);
    const float bb = bf2f(b2v);
    u16* op = out + (size_t)(rb * 64 + w * 8) * D_ + cb * 64 + lane;
#pragma unroll
    for (int j = 0; j < 4; ++j) {
      const float a0 = j < 2 ? v0[2 * j] : v1[2 * j - 4], a1 = j < 2 ? v0[2 * j + 1] : v1[2 * j - 3];
      const unsigned pa = pk_bf16(a0, a1);
      const unsigned pv = pk_bf16(bflo(pa) + bb, bfhi(pa) + bb);
      const float q0 = bflo(pv), q1 = bfhi(pv);
      const unsigned pg = pk_bf16(q0 * __builtin_amdgcn_rcpf(1.f + __builtin_amdgcn_exp2f(q0 * -1.44269504f)),
                                  q1 * __builtin_amdgcn_rcpf(1.f + __builtin_amdgcn_exp2f(q1 * -1.44269504f)));
      const unsigned ps = pk_bf16(bflo(sv[j]) * bflo(pg), bfhi(sv[j]) * bfhi(pg));
      const unsigned po = pk_bf16(bf2f(xv[2 * j]) + bflo(ps), bf2f(xv[2 * j + 1]) + bfhi(ps));
      op[(size_t)(2 * j) * D_] = (u16)po;
      op[(size_t)(2 * j + 1) * D_] = (u16)(po >> 16);
    }
  }
}

extern "C" void kernel_launch(void* const* d_in, const int* in_sizes, int n_in, void* d_out, int out_size, void* d_ws,
                              size_t ws_size, hipStream_t stream) {
  (void)in_sizes; (void)n_in; (void)out_size; (void)ws_size;
  (void)hipFuncSetAttribute((const void*)k1_ffn_up, hipFuncAttributeMaxDynamicSharedMemorySize, K1_LDS);
  (void)hipFuncSetAttribute((const void*)k2_down_conv, hipFuncAttributeMaxDynamicSharedMemorySize, K2_LDS);
  const u16* x = (const u16*)d_in[0];
  k1_ffn_up<<<256, 576, K1_LDS, stream>>>(x, (const u16*)d_in[8], (const u16*)d_in[9], (const u16*)d_in[10],
                                          (const u16*)d_in[1], (const u16*)d_in[2], (const u16*)d_in[3],
                                          (const u16*)d_in[4], (const u16*)d_in[5], (char*)d_ws);
  k2_down_conv<<<256, 512, K2_LDS, stream>>>(x, (const u16*)d_in[6], (const u16*)d_in[11], (char*)d_ws, (u16*)d_out);
}
```

```cpp
#include <hip/hip_runtime.h>
#include <stdint.h>

typedef unsigned short u16;
typedef __attribute__((ext_vector_type(8))) __bf16 bf16x8;
typedef __attribute__((ext_vector_type(2))) __bf16 bf16x2;
typedef __attribute__((ext_vector_type(8))) _Float16 f16x8;
typedef __attribute__((ext_vector_type(2))) _Float16 f16x2;
typedef __attribute__((ext_vector_type(4))) short s16x4;
typedef __attribute__((ext_vector_type(2))) float f32x2;
typedef __attribute__((ext_vector_type(4))) float f32x4;
typedef __attribute__((ext_vector_type(16))) float f32x16;
typedef __attribute__((ext_vector_type(4))) unsigned u32x4;
typedef __attribute__((ext_vector_type(2))) unsigned u32x2;
typedef __attribute__((ext_vector_type(8))) unsigned u32x8;
typedef __attribute__((ext_vector_type(16))) __bf16 bf16x16;
#define LAS __attribute__((address_space(3)))
#define GAS __attribute__((address_space(1)))
#if defined(__HIP_DEVICE_COMPILE__)
#define NOPK __attribute__((target("no-packed-fp32-ops")))
#else
#define NOPK
#endif
#define DI __device__ __forceinline__ NOPK

constexpr int L_ = 2048, D_ = 512, N_ = 128, F_ = 1792;

DI float u2f(unsigned v) { return __builtin_bit_cast(float, v); }
DI unsigned f2u(float v) { return __builtin_bit_cast(unsigned, v); }
DI bool wave_any(bool p) { return __builtin_amdgcn_ballot_w64(p) != 0ull; }
DI float bf2f(u16 v) { return u2f(((unsigned)v) << 16); }
DI float bflo(unsigned v) { return u2f(v << 16); }
DI float bfhi(unsigned v) { return u2f(v & 0xffff0000u); }
DI unsigned pk_bf16(float a, float b) { f32x2 v = {a, b}; return __builtin_bit_cast(unsigned, __builtin_convertvector(v, bf16x2)); }
DI unsigned pk_f16(float a, float b) { f32x2 v = {a, b}; return __builtin_bit_cast(unsigned, __builtin_convertvector(v, f16x2)); }
DI unsigned pk_f16v(f32x2 v) { return __builtin_bit_cast(unsigned, __builtin_convertvector(v, f16x2)); }
DI bf16x2 as_bf2(unsigned v) { return __builtin_bit_cast(bf16x2, v); }
DI f16x2 as_h2(unsigned v) { return __builtin_bit_cast(f16x2, v); }
DI bf16x8 as_bf8(u32x4 v) { return __builtin_bit_cast(bf16x8, v); }
DI f16x8 as_h8(u32x4 v) { return __builtin_bit_cast(f16x8, v); }
DI float rbf(float a) { return bflo(pk_bf16(a, a)); }
DI unsigned bfbits(float a) { return pk_bf16(a, a) & 0xffffu; }
DI float both_halves(float v) {
  auto r = __builtin_amdgcn_permlane32_swap(f2u(v), f2u(v), false, false);
  return u2f(r[0]) + u2f(r[1]);
}
DI void lo_hi(float v, float& lo, float& hi) {
  auto r = __builtin_amdgcn_permlane32_swap(f2u(v), f2u(v), false, false);
  lo = u2f(r[0]); hi = u2f(r[1]);
}
DI float wave_absmax(float x) {
  unsigned v = f2u(x);
  v = max(v, (unsigned)__builtin_amdgcn_update_dpp(0, v, 0xB1, 0xf, 0xf, true));
  v = max(v, (unsigned)__builtin_amdgcn_update_dpp(0, v, 0x4E, 0xf, 0xf, true));
  v = max(v, (unsigned)__builtin_amdgcn_update_dpp(0, v, 0x141, 0xf, 0xf, true));
  v = max(v, (unsigned)__builtin_amdgcn_update_dpp(0, v, 0x140, 0xf, 0xf, true));
  unsigned a = __builtin_amdgcn_readlane(v, 0), b = __builtin_amdgcn_readlane(v, 16), c = __builtin_amdgcn_readlane(v, 32),
           d = __builtin_amdgcn_readlane(v, 48);
  return u2f(max(max(a, b), max(c, d)));
}
DI float wave_sum(float x) {
  x += u2f(__builtin_amdgcn_update_dpp(0, f2u(x), 0xB1, 0xf, 0xf, true));
  x += u2f(__builtin_amdgcn_update_dpp(0, f2u(x), 0x4E, 0xf, 0xf, true));
  x += u2f(__builtin_amdgcn_update_dpp(0, f2u(x), 0x141, 0xf, 0xf, true));
  x += u2f(__builtin_amdgcn_update_dpp(0, f2u(x), 0x140, 0xf, 0xf, true));
  const unsigned v = f2u(x);
  return (u2f(__builtin_amdgcn_readlane(v, 0)) + u2f(__builtin_amdgcn_readlane(v, 16))) +
         (u2f(__builtin_amdgcn_readlane(v, 32)) + u2f(__builtin_amdgcn_readlane(v, 48)));
}
DI float next_lane(float v) { return u2f(__builtin_amdgcn_update_dpp(0, f2u(v), 0x130, 0xf, 0xf, true)); }
DI float rdlane(float v, int l) { return u2f(__builtin_amdgcn_readlane(f2u(v), l)); }

#define WAIT_VM(n) asm volatile("s_waitcnt vmcnt(" #n ")" ::: "memory")
#define SCHED() __builtin_amdgcn_sched_barrier(0)
#define WAIT_LGKM0() asm volatile("s_waitcnt lgkmcnt(0)" ::: "memory")
#define BARRIER() do { SCHED(); asm volatile("s_waitcnt lgkmcnt(0)" ::: "memory"); __builtin_amdgcn_s_barrier(); asm volatile("" ::: "memory"); SCHED(); } while (0)

constexpr size_t WS_HF = 0;
constexpr size_t WS_US = WS_HF + (size_t)64 * 112 * 1024;
constexpr size_t WS_UT = WS_US + (size_t)512 * 4096;
constexpr size_t WS_GR = WS_UT + (size_t)512 * 4096;
constexpr size_t WS_EP = WS_GR + (size_t)2048 * 16;
constexpr size_t WS_FU = WS_EP + 4096;
constexpr size_t WS_FS = WS_FU + 8192;
constexpr size_t WS_FH = WS_FS + 8192;
constexpr size_t WS_W2F = WS_FH + 32768;

DI f32x2 cexp_p(float zr2, float zi, float p) {
  float th = zi * p;
  float n = __builtin_rintf(th * 0.15915494f);
  float rr = fmaf(th, 0.15915494f, -n);
  rr = fmaf(th, 6.4206382e-9f, rr);
  float mag = __builtin_amdgcn_exp2f(zr2 * p);
  return (f32x2){mag * __builtin_amdgcn_cosf(rr), mag * __builtin_amdgcn_sinf(rr)};
}
DI float half_wave_max(float x) {
  unsigned v = f2u(x);
  v = max(v, (unsigned)__builtin_amdgcn_update_dpp(0, v, 0xB1, 0xf, 0xf, true));
  v = max(v, (unsigned)__builtin_amdgcn_update_dpp(0, v, 0x4E, 0xf, 0xf, true));
  v = max(v, (unsigned)__builtin_amdgcn_update_dpp(0, v, 0x141, 0xf, 0xf, true));
  v = max(v, (unsigned)__builtin_amdgcn_update_dpp(0, v, 0x140, 0xf, 0xf, true));
  auto r = __builtin_amdgcn_permlane16_swap(v, v, false, false);
  return u2f(max((unsigned)r[0], (unsigned)r[1]));
}
DI float sel16(u32x2 v, int p) { const unsigned d = (p & 2) ? v[1] : v[0]; return u2f((p & 1) ? (d & 0xffff0000u) : (d << 16)); }
#define ROT cb
#define GELU_PRIO 0
#define AWN 4
#define BWN 7
#define S_GO 4
#define G2_GROUP(i) ((i) < 0 ? 0 : (((i) + BWN < 16 ? 2 : 0) + ((i) + AWN < 16 ? 2 : 0) + ((i) == 16 - AWN ? 9 : 0)))
#define g2_behind(k) (((k) < AWN ? 2 * (AWN - 1 - (k)) : 0) + (1 < AWN ? G2_GROUP((k) - 1) : 0) + (2 < AWN ? G2_GROUP((k) - 2) : 0) + \
                      (3 < AWN ? G2_GROUP((k) - 3) : 0) + (4 < AWN ? G2_GROUP((k) - 4) : 0) + (5 < AWN ? G2_GROUP((k) - 5) : 0))
#define SVC_PRIO2 3
constexpr int STG = 36864;
constexpr int K1_W2 = 4 * STG;
constexpr int K1_MS = K1_W2 + 7168;
constexpr int K1_MF = K1_MS + 256;
constexpr int K1_FL = K1_MF + 512;
constexpr int K1_KS = K1_FL + 96;
constexpr int K1_LDS = 163840;
constexpr int S_TILE = K1_KS + 32;
constexpr int TAB = 53 * 272;
constexpr int CP_STRIDE = 4352 + 64;
constexpr int CV_CP = 0;
constexpr int CV_U = 4 * CP_STRIDE;
constexpr int CV_XY = CV_U + 4608;

DI void dma1(unsigned lds, const char* xb, unsigned vx) {
  asm volatile("s_mov_b32 m0, %2\n\ts_nop 0\n\tglobal_load_lds_dwordx4 %0, %1" ::"v"(vx), "s"(xb), "s"(lds) : "memory");
}
DI void dma2(unsigned lds, const char* wb, unsigned v0, unsigned v1) {
  asm volatile(
      "s_mov_b32 m0, %3\n\ts_nop 0\n\t"
      "global_load_lds_dwordx4 %0, %2\n\t"
      "global_load_lds_dwordx4 %1, %2 offset:1024" ::"v"(v0), "v"(v1), "s"(wb), "s"(lds)
      : "memory");
}
DI u32x2 tr_rd(const char* p) { return __builtin_bit_cast(u32x2, __builtin_amdgcn_ds_read_tr16_b64_v4i16((LAS s16x4*)p)); }
DI void st_wt(char* p, u32x4 v) {
  asm volatile("global_store_dwordx4 %0, %1, off sc1\n\ts_nop 1" ::"v"(p), "v"(v) : "memory");
}
DI void st_wt2(char* p, u32x2 v) { asm volatile("global_store_dwordx2 %0, %1, off sc1" ::"v"(p), "v"(v) : "memory"); }
DI void st_pl(char* p, u32x4 v) { asm volatile("global_store_dwordx4 %0, %1, off\n\ts_nop 1" ::"v"(p), "v"(v) : "memory"); }
DI void lds_set(LAS unsigned* p, unsigned v) {
  WAIT_LGKM0();
  __hip_atomic_store(p, v, __ATOMIC_RELAXED, __HIP_MEMORY_SCOPE_WORKGROUP);
  asm volatile("" ::: "memory");
}
DI void lds_wait(LAS unsigned* p, unsigned v) {
  while ((unsigned)__builtin_amdgcn_readfirstlane(__hip_atomic_load(p, __ATOMIC_RELAXED, __HIP_MEMORY_SCOPE_WORKGROUP)) < v)
    __builtin_amdgcn_s_sleep(0);
  asm volatile("" ::: "memory");
}
DI void lds_arrive(unsigned addr) {
  const unsigned one = 1u;
  asm volatile("s_waitcnt lgkmcnt(0)\n\ts_mov_b64 exec, 1\n\tds_add_u32 %0, %1\n\ts_mov_b64 exec, -1" ::"v"(addr), "v"(one) : "memory");
}
DI f32x2 cmul(f32x2 a, f32x2 b) { return (f32x2){fmaf(-a[1], b[1], a[0] * b[0]), fmaf(a[1], b[0], a[0] * b[1])}; }
DI unsigned mix(unsigned lo, unsigned hi) { return (lo & 0xffffu) | (hi & 0xffff0000u); }
DI unsigned nxt(unsigned v) { return __builtin_amdgcn_update_dpp(0, v, 0x130, 0xf, 0xf, true); }

DI float row_sum16(float x) {
  x += u2f(__builtin_amdgcn_update_dpp(0, f2u(x), 0xB1, 0xf, 0xf, true));
  x += u2f(__builtin_amdgcn_update_dpp(0, f2u(x), 0x4E, 0xf, 0xf, true));
  x += u2f(__builtin_amdgcn_update_dpp(0, f2u(x), 0x141, 0xf, 0xf, true));
  x += u2f(__builtin_amdgcn_update_dpp(0, f2u(x), 0x140, 0xf, 0xf, true));
  return x;
}
DI float row_sum32(float x) {
  x = row_sum16(x);
  auto r = __builtin_amdgcn_permlane16_swap(f2u(x), f2u(x), false, false);
  return u2f(r[0]) + u2f(r[1]);
}
DI void ln_stats(const u32x4 (&xr)[2], u32x2 ep, char* ws, int lane, int token0, char* dst) {
  float sm[2], q[4];
#pragma unroll
  for (int i = 0; i < 2; ++i)
    sm[i] = ((bflo(xr[i][0]) + bfhi(xr[i][0])) + (bflo(xr[i][1]) + bfhi(xr[i][1]))) +
            ((bflo(xr[i][2]) + bfhi(xr[i][2])) + (bflo(xr[i][3]) + bfhi(xr[i][3])));
  const float mu = row_sum32(sm[0] + sm[1]) * (1.f / 512.f);
#pragma unroll
  for (int j = 0; j < 4; ++j) {
    q[j] = 0.f;
#pragma unroll
    for (int i = 0; i < 2; ++i) {
      const float d0 = bflo(xr[i][j]) - mu, d1 = bfhi(xr[i][j]) - mu;
      q[j] = fmaf(d1, d1, fmaf(d0, d0, q[j]));
    }
  }
  const float var = row_sum32((q[0] + q[1]) + (q[2] + q[3])) * (1.f / 512.f);
  const unsigned mb = bfbits(mu);
  const unsigned sb = bfbits(__builtin_amdgcn_sqrtf(rbf(rbf(var) + 1e-5f)));
  const unsigned long long tag = ((((unsigned long long)ep[1] << 32) | ep[0]) + 1ull) << 16;
  const u32x4 gr = {(unsigned)tag | mb, (unsigned)(tag >> 32), (unsigned)tag | sb, (unsigned)(tag >> 32)};
  if ((lane & 31) == 0) st_wt(ws + WS_GR + (size_t)(token0 + (lane >> 5)) * 16, gr);
  const float m_ = bflo(mb), rho = __builtin_amdgcn_rcpf(bflo(sb));
#pragma unroll
  for (int i = 0; i < 2; ++i) {
    u32x4 o;
#pragma unroll
    for (int j = 0; j < 4; ++j) {
      const unsigned d = pk_bf16(bflo(xr[i][j]) - m_, bfhi(xr[i][j]) - m_);
      const unsigned n = pk_bf16(bflo(d) * rho, bfhi(d) * rho);
      o[j] = pk_f16(bflo(n), bfhi(n));
    }
    *(u32x4*)(dst + i * 512) = o;
  }
}
DI unsigned norm2(float x0, float x1, float m0, float m1, float r0, float r1) {
  const unsigned d = pk_bf16(x0 - m0, x1 - m1);
  const unsigned n = pk_bf16(bflo(d) * r0, bfhi(d) * r1);
  return pk_f16(bflo(n), bfhi(n));
}

#define PUT(row, v) *(unsigned*)(tab + (row) * 272) = pk_f16v(v)
DI void tab_gen(char* tab, f32x2 cc, f32x2 e1, f32x2 w8, f32x2 G) {
      const f32x2 w16 = cmul(w8, w8), e2 = cmul(e1, e1), e4 = cmul(e2, e2);
      f32x2 R = cc, Ee = w16, Eo = w8, Gq = cmul(cc, G), T = Gq;
      PUT(0, R); *(unsigned*)(tab + 21 * 272) = 0x00003C00u; PUT(22, Eo); PUT(23, Ee); PUT(8, Gq);
#pragma unroll
      for (int k = 1; k < 16; ++k) {
        Eo = cmul(Eo, w16); PUT(22 + 2 * k, Eo);
        if (k < 15) { Ee = cmul(Ee, w16); PUT(23 + 2 * k, Ee); }
        if ((k & 1) && k < 15) { R = cmul(R, e1); PUT((k + 1) >> 1, R); }
        if (!(k & 1)) {
          const int g = k >> 1;
          Gq = cmul(Gq, G);
          if (g <= 2) { PUT(10 + 2 * g, Gq); const f32x2 q = cmul(Gq, e4); PUT(11 + 2 * g, q); }
          else PUT(13 + g, Gq);
        }
        if (k >= 7 && k < 10) { T = cmul(T, e2); PUT(2 + k, T); }
      }
}
DI void conv_run(char* cv, char* ws, LAS unsigned* fl, int c, int cw, int lane, float kun, float dsk, unsigned long long en) {
    const int hf = cw & 1;
    {
      u32x4 lo[3];
      u32x2 hi[3];
      int chs[3];
#pragma unroll
      for (int rd = 0; rd < 3; ++rd) {
        const int idx = min(rd * 64 + lane, 143);
        const int k1 = idx / 18;
        chs[rd] = (16 * hf + (idx - 18 * k1) + 32 * k1) * 16;
      }
      WAIT_LGKM0();
#pragma unroll
      for (int rd = 0; rd < 3; ++rd) {
        lo[rd] = *(const u32x4*)(cv + CV_CP + chs[rd]);
        hi[rd] = *(const u32x2*)(cv + CV_CP + chs[rd] + 16);
      }
#pragma unroll
      for (int rd = 0; rd < 3; ++rd) {
        const unsigned d[6] = {lo[rd][0], lo[rd][1], lo[rd][2], lo[rd][3], hi[rd][0], hi[rd][1]};
        unsigned al[5];
#pragma unroll
        for (int i = 0; i < 5; ++i) al[i] = __builtin_amdgcn_alignbit(d[i + 1], d[i], 16);
        char* dst = cv + CV_CP + chs[rd];
        *(u32x4*)(dst + CP_STRIDE) = (u32x4){al[0], al[1], al[2], al[3]};
        *(u32x4*)(dst + 2 * CP_STRIDE) = (u32x4){d[1], d[2], d[3], d[4]};
        *(u32x4*)(dst + 3 * CP_STRIDE) = (u32x4){al[1], al[2], al[3], al[4]};
      }
    }
    WAIT_LGKM0();
    const int i16 = lane & 15, g4 = lane >> 4, a1 = i16 + 1;
    const LAS char* ap = (const LAS char*)(size_t)(unsigned)(size_t)(cv + CV_CP + (a1 & 3) * CP_STRIDE + (16 * hf + g4) * 16 + (a1 >> 2) * 8);
    const char* bp = cv + CV_U + (2032 + 128 * hf - 16 * i16 + 8 * g4) * 2;
    f32x4 ya[8];
#pragma unroll
    for (int t = 0; t < 8; ++t) ya[t] = (f32x4){0.f, 0.f, 0.f, 0.f};
    {
      u32x4 A[8], B[8];
#define RD_A(k0, i) do { const u32x2 q0 = *(const volatile LAS u32x2*)(ap + (k0) * 64 + (i) * 512), q1 = *(const volatile LAS u32x2*)(ap + (k0) * 64 + (i) * 512 + 8); \
        A[i] = (u32x4){q0[0], q0[1], q1[0], q1[1]}; } while (0)
#define RD_B(k0, i) B[i] = *(const u32x4*)(bp + (k0) * 64 - (i) * 512)
#pragma unroll
      for (int i = 0; i < 8; ++i) { RD_A(0, i); RD_B(0, i); }
      SCHED();
#pragma unroll
      for (int k0 = 0; k0 < 4; ++k0) {
#pragma unroll
        for (int dl = 0; dl < 8; ++dl) {
#pragma unroll
          for (int k1 = 0; k1 + dl < 8; ++k1)
            ya[k1 + dl] = __builtin_amdgcn_mfma_f32_16x16x32_f16(as_h8(A[k1]), as_h8(B[dl]), ya[k1 + dl], 0, 0, 0);
          SCHED();
          if (k0 < 3) { RD_B(k0 + 1, dl); RD_A(k0 + 1, 7 - dl); }
          SCHED();
        }
      }
    }
    {
      char* xy = cv + CV_XY + hf * 4096 + lane * 16;
#pragma unroll
      for (int t = 0; t < 8; ++t) asm volatile("" : "+v"(ya[t]));
      f32x4 mine[4];
      if (hf) {
#pragma unroll
        for (int t = 0; t < 4; ++t) { *(f32x4*)(xy + t * 1024) = ya[t]; mine[t] = ya[4 + t]; }
      } else {
#pragma unroll
        for (int t = 0; t < 4; ++t) { *(f32x4*)(xy + t * 1024) = ya[4 + t]; mine[t] = ya[t]; }
      }
      lds_set(fl + 6 + cw, 1u);
            const int tb0 = 1024 * hf + 16 * i16 + 4 * g4;
      unsigned xd[4][2];
#pragma unroll
      for (int t = 0; t < 4; ++t) {
        const u32x2 uu = *(const u32x2*)(cv + CV_U + (2044 - tb0 - 256 * t) * 2);
        const f16x2 ua = as_h2(uu[0]), ub = as_h2(uu[1]);
        xd[t][0] = pk_bf16((float)ua[0] * dsk, (float)ua[1] * dsk);
        xd[t][1] = pk_bf16((float)ub[0] * dsk, (float)ub[1] * dsk);
      }
      lds_wait(fl + 6 + (cw ^ 1), 1u);
      const char* xo = cv + CV_XY + (hf ^ 1) * 4096 + lane * 16;
      char* sp = ws + WS_US + (size_t)((tb0 >> 6) * 512 + c) * 128 + (60 - (tb0 & 63)) * 2;
#pragma unroll
      for (int t = 0; t < 4; ++t) {
        const f32x4 y = (*(const f32x4*)(xo + t * 1024) + mine[t]) * kun;
        const unsigned y0 = pk_bf16(y[3], y[2]), y1 = pk_bf16(y[1], y[0]);
        const u32x2 sv = {pk_bf16(bflo(y0) + bflo(xd[t][0]), bfhi(y0) + bfhi(xd[t][0])), pk_bf16(bflo(y1) + bflo(xd[t][1]), bfhi(y1) + bfhi(xd[t][1]))};
        st_wt2(sp + (size_t)t * 4 * 512 * 128, sv);
      }
      lds_arrive((unsigned)(size_t)(fl + 11));
      WAIT_VM(0);
      if (lane == 0)
        __hip_atomic_store((GAS unsigned long long*)(ws + WS_FS + (c * 2 + hf) * 8), en, __ATOMIC_RELAXED, __HIP_MEMORY_SCOPE_AGENT);
    }
}

__global__ __launch_bounds__(704, 3) NOPK void k1_ffn_up(const u16* __restrict__ x, const u16* __restrict__ w1,
                                                        const u16* __restrict__ b1, const u16* __restrict__ w2,
                                                        const u16* __restrict__ lar,
                                                        const u16* __restrict__ aim, const u16* __restrict__ cre,
                                                        const u16* __restrict__ cim, const u16* __restrict__ ldt,
                                                        const u16* __restrict__ dskip, const u16* __restrict__ b2,
                                                        char* __restrict__ ws, u16* __restrict__ out) {
  extern __shared__ __attribute__((aligned(16))) char smem[];
  const int tid = threadIdx.x, lane = tid & 63;
  const int w = __builtin_amdgcn_readfirstlane(tid >> 6);
  const int b = blockIdx.x, xcd = b & 7, jj = b >> 3;
  const int rb = (xcd >> 1) * 8 + (jj & 7), cb = (xcd & 1) * 4 + (jj >> 3);
  const int r = lane & 31, h = lane >> 5;
  const unsigned lds0 = (unsigned)(size_t)smem;
  const char* xb = (const char*)x;
  const char* wb = (const char*)w1;
  LAS unsigned* fl = (LAS unsigned*)(smem + K1_FL);
  const int blk = (lane >> 4) & 1, q4 = (lane & 15) >> 2, p4 = lane & 3;

  const int fw = w;
  unsigned xv[8], b2v;
#define LOAD_FINAL() do { const char* xp_ = (const char*)(x + (size_t)(rb * 64 + fw * 8) * D_ + cb * 64); const unsigned l2_ = lane * 2, l3_ = 4096 + lane * 2; \
    asm volatile("global_load_ushort %0, %9, %11\n\tglobal_load_ushort %1, %9, %11 offset:1024\n\tglobal_load_ushort %2, %9, %11 offset:2048\n\t" \
                 "global_load_ushort %3, %9, %11 offset:3072\n\tglobal_load_ushort %4, %10, %11\n\tglobal_load_ushort %5, %10, %11 offset:1024\n\t" \
                 "global_load_ushort %6, %10, %11 offset:2048\n\tglobal_load_ushort %7, %10, %11 offset:3072\n\tglobal_load_ushort %8, %9, %12" \
                 : "=&v"(xv[0]), "=&v"(xv[1]), "=&v"(xv[2]), "=&v"(xv[3]), "=&v"(xv[4]), "=&v"(xv[5]), "=&v"(xv[6]), "=&v"(xv[7]), "=&v"(b2v) \
                 : "v"(l2_), "v"(l3_), "s"(xp_), "s"(b2 + cb * 64) : "memory"); } while (0)
#define WAIT_FINAL() asm volatile("s_waitcnt vmcnt(0)" : "+v"(xv[0]), "+v"(xv[1]), "+v"(xv[2]), "+v"(xv[3]), "+v"(xv[4]), "+v"(xv[5]), "+v"(xv[6]), "+v"(xv[7]), "+v"(b2v)::"memory")
#define RED_OFF(q) ((q) * 16384)
  if (w < 7) {
    const unsigned vx = (unsigned)((rb * 64 + w * 8 + (lane >> 3)) * 1024 + (((lane & 7) ^ (((w & 1) * 4 + (lane >> 4)) & 7)) << 4));
    const unsigned vx7 = vx + 32 * 1024;
    const unsigned ldx = lds0 + w * 1024;
    const bool extra = (w == 3);
#define XDMA(s) do { const char* xs_ = xb + (((s) + ROT) & 7) * 128; dma1(ldx + ((s) & 3) * STG, xs_, vx); \
                     if (extra) dma1(lds0 + 7168 + ((s) & 3) * STG, xs_, vx7); } while (0)
    XDMA(0);
    unsigned vw[4];
#pragma unroll
    for (int m = 0; m < 4; ++m) {
      int c = (4 * w + m) * 64 + lane, row = c / 28, cc = c - row * 28;
      vw[m] = (unsigned)(row * 3584 + cb * 448 + cc * 16 - m * 1024);
    }
    const unsigned ldw = lds0 + 8192 + w * 4096;
#define WDMA_A(d) dma2(ldw + ((d) & 3) * STG, wb + (size_t)(((d) + ROT) & 7) * (64 * 3584), vw[0], vw[1])
#define WDMA_B(d) dma2(ldw + ((d) & 3) * STG + 2048, wb + (size_t)(((d) + ROT) & 7) * (64 * 3584) + 2048, vw[2], vw[3])
    WDMA_A(0); WDMA_B(0);
    BARRIER();
    XDMA(1); WDMA_A(1); WDMA_B(1);
    unsigned b1v;
#define NPF 1
    { const unsigned vo = r * 2; asm volatile("global_load_ushort %0, %1, %2" : "=&v"(b1v) : "v"(vo), "s"(b1 + cb * 224 + w * 32) : "memory"); }
    const unsigned aoff = 8192 + (8 * h + q4) * 448 + (w * 32 + 16 * blk + 4 * p4) * 2;
    unsigned boff[4];
#pragma unroll
    for (int k = 0; k < 4; ++k) boff[k] = r * 128 + (((2 * k + h) ^ ((r >> 1) & 7)) << 4);
    f32x16 acc[2];
#pragma unroll
    for (int i = 0; i < 16; ++i) { acc[0][i] = 0.f; acc[1][i] = 0.f; }
    const int row16 = lane & 15;
    const unsigned one2 = 0x3F803F80u;
    const u32x4 sa = {row16 < 2 ? one2 : 0u, row16 < 2 ? one2 : 0u, (row16 & 14) == 2 ? one2 : 0u, (row16 & 14) == 2 ? one2 : 0u};
    const int sidx = (lane & 1) ? 0xEEEEEEEE : 0x44444444;
    f32x4 csa = {0.f, 0.f, 0.f, 0.f};
    u32x8 A[2][2];
    u32x4 B0[2][4], B1[2][4];
    unsigned ms[2];
#define MM(k, which) do { const u32x8 a8 = A[p][(k) >> 1]; \
      const u32x4 a4 = ((k) & 1) ? (u32x4){a8[4], a8[5], a8[6], a8[7]} : (u32x4){a8[0], a8[1], a8[2], a8[3]}; \
      if (which == 0) acc[0] = __builtin_amdgcn_mfma_f32_32x32x16_bf16(as_bf8(a4), as_bf8(B0[p][k]), acc[0], 0, 0, 0); \
      else { acc[1] = __builtin_amdgcn_mfma_f32_32x32x16_bf16(as_bf8(a4), as_bf8(B1[p][k]), acc[1], 0, 0, 0); \
             if ((k) & 1) csa = __builtin_amdgcn_smfmac_f32_16x16x64_bf16(as_bf8(sa), __builtin_bit_cast(bf16x16, a8), csa, sidx, 0, 0); } \
      SCHED(); } while (0)
#define RD(k) do { const u32x2 al = tr_rd(sb + aoff + (16 * (k)) * 448), ah = tr_rd(sb + aoff + (16 * (k) + 4) * 448); \
      A[s & 1][(k) >> 1][4 * ((k) & 1)] = al[0]; A[s & 1][(k) >> 1][4 * ((k) & 1) + 1] = al[1]; \
      A[s & 1][(k) >> 1][4 * ((k) & 1) + 2] = ah[0]; A[s & 1][(k) >> 1][4 * ((k) & 1) + 3] = ah[1]; \
      B0[s & 1][k] = *(const u32x4*)(sb + boff[k]); B1[s & 1][k] = *(const u32x4*)(sb + 4096 + boff[k]); SCHED(); } while (0)
#pragma unroll
    for (int s = 0; s <= 8; ++s) {
      const char* sb = smem + (s & 3) * STG;
      const int p = (s - 1) & 1;
      const bool dm = (s >= 1 && s + 3 <= 7);
      const int d = (s == 1) ? 3 : s + 3;
      if (s < 8) {
        if (s <= 1) asm volatile("s_waitcnt vmcnt(%0)" ::"i"(5 + NPF) : "memory");
        else if (s == 2) asm volatile("s_waitcnt vmcnt(10)" : "+v"(b1v)::"memory");
        else if (s <= 5) WAIT_VM(10); else if (s == 6) WAIT_VM(5); else WAIT_VM(0);
        lds_arrive(lds0 + K1_FL);
        SCHED();
      }
      if (s == 8) lds_arrive(lds0 + K1_FL + 40);
      if (s >= 1) { MM(0, 0); MM(0, 1); }
      if (s < 8) {
        lds_wait(fl, 7 * (s + 1));
        SCHED();
        RD(0); RD(1);
      }
      if (s >= 1) MM(1, 0);
      if (dm) XDMA(d);
      if (s >= 1) MM(1, 1);
      if (s < 8) RD(2);
      if (s >= 1) MM(2, 0);
      if (dm) WDMA_A(d);
      if (s >= 1) MM(2, 1);
      if (s < 8) RD(3);
      if (s >= 1) MM(3, 0);
      if (dm) WDMA_B(d);
      if (s >= 1) MM(3, 1);
      if (s == 0) { XDMA(2); WDMA_A(2); WDMA_B(2); }
      if (s == 1) { XDMA(4); WDMA_A(4); WDMA_B(4); }
      if (s == 7) {
        lds_wait(fl + 1, 1u);
        ms[0] = *(const unsigned*)(smem + K1_MS + r * 4);
        ms[1] = *(const unsigned*)(smem + K1_MS + 128 + r * 4);
      }
      SCHED();
    }
    __builtin_amdgcn_s_setprio(GELU_PRIO);
    const float cs = u2f(__builtin_amdgcn_permlane16_swap(f2u(csa[0] + csa[1]), f2u(csa[2] + csa[3]), false, false)[0]);
    const float hi = rbf(cs), r1 = cs - hi, mid = rbf(r1), lo = r1 - mid;
    u32x4 a = {pk_bf16(hi, mid), bfbits(lo) | (b1v << 16), 0u, 0u};
    if (h) { a[0] = 0u; a[1] = 0u; }
#pragma unroll
    for (int t = 0; t < 2; ++t) {
      const unsigned nm = (ms[t] & 0xffffu) ^ 0x8000u;
      const u32x4 bb = {nm | (nm << 16), nm | (ms[t] & 0xffff0000u), 0u, 0u};
      acc[t] = __builtin_amdgcn_mfma_f32_32x32x16_bf16(as_bf8(a), as_bf8(bb), acc[t], 0, 0, 0);
    }
#pragma unroll
    for (int t = 0; t < 2; ++t) {
      const float sdv = bfhi(ms[t]), rho = __builtin_amdgcn_rcpf(sdv);
      const float ca = -2.302208198f * rho, cbb = -0.1029432397f * rho * rho * rho;
      char* hp = ws + WS_HF + ((size_t)((rb * 2 + t) * 112 + cb * 14 + 2 * w) * 64 + lane) * 16;
#pragma unroll
      for (int hh = 0; hh < 2; ++hh) {
        unsigned o[4];
#pragma unroll
        for (int i = 0; i < 4; ++i) {
          float g[2];
#pragma unroll
          for (int e = 0; e < 2; ++e) {
            float d = acc[t][8 * hh + 2 * i + e];
            float ex = __builtin_amdgcn_exp2f(d * fmaf(d * d, cbb, ca));
            g[e] = d * __builtin_amdgcn_rcpf(fmaf(ex, sdv, sdv));
          }
          o[i] = pk_bf16(g[0], g[1]);
        }
        st_wt(hp + hh * 1024, (u32x4){o[0], o[1], o[2], o[3]});
      }
    }
    __builtin_amdgcn_s_setprio(0);
    const unsigned long long en = *(const unsigned long long*)(smem + K1_KS + 16);
    WAIT_VM(0);
    {
      unsigned old = 0;
      if (lane == 0) old = __hip_atomic_fetch_add(fl + 14, 1u, __ATOMIC_RELAXED, __HIP_MEMORY_SCOPE_WORKGROUP);
      if (__builtin_amdgcn_readfirstlane(old) == 6u && lane == 0)
        __hip_atomic_store((GAS unsigned long long*)(ws + WS_FH + (rb * 8 + cb) * 128), en, __ATOMIC_RELAXED, __HIP_MEMORY_SCOPE_AGENT);
    }
    {
      const unsigned l16 = lane * 16, l17 = 112 * 1024 + lane * 16;
      if (w == 0) {
        const unsigned fo = (unsigned)WS_FU + l16;
        u32x4 f[8];
#pragma unroll 1
        for (unsigned spins = 0; spins < (1u << 20); ++spins) {
          asm volatile("global_load_dwordx4 %0, %8, %10 sc1\n\tglobal_load_dwordx4 %1, %8, %10 offset:1024 sc1\n\t"
                       "global_load_dwordx4 %2, %8, %10 offset:2048 sc1\n\tglobal_load_dwordx4 %3, %8, %10 offset:3072 sc1\n\t"
                       "global_load_dwordx4 %4, %9, %10 sc1\n\tglobal_load_dwordx4 %5, %9, %10 offset:1024 sc1\n\t"
                       "global_load_dwordx4 %6, %9, %10 offset:2048 sc1\n\tglobal_load_dwordx4 %7, %9, %10 offset:3072 sc1\n\ts_waitcnt vmcnt(0)"
                       : "=&v"(f[0]), "=&v"(f[1]), "=&v"(f[2]), "=&v"(f[3]), "=&v"(f[4]), "=&v"(f[5]), "=&v"(f[6]), "=&v"(f[7])
                       : "v"(fo), "v"(fo + 4096), "s"(ws) : "memory");
          bool bad = false;
#pragma unroll
          for (int i = 0; i < 8; ++i)
            bad = bad || f[i][0] != (unsigned)en || f[i][1] != (unsigned)(en >> 32) || f[i][2] != (unsigned)en || f[i][3] != (unsigned)(en >> 32);
          if (!wave_any(bad)) break;
          __builtin_amdgcn_s_sleep(4);
        }
        lds_set(fl + 15, 1u);
        if (b == 0 && lane == 0)
          __hip_atomic_store((GAS unsigned long long*)(ws + WS_EP), en, __ATOMIC_RELAXED, __HIP_MEMORY_SCOPE_AGENT);
      } else {
        lds_wait(fl + 15, 1u);
      }
      const char* ha = ws + WS_HF + (size_t)((rb * 2) * 112 + 16 * w) * 1024;
      const char* wp = ws + WS_W2F + (size_t)((cb * 2) * 112 + 16 * w) * 1024;
      u32x4 fa[AWN][2], fb[BWN][2];
#define B_ISSUE(k) asm volatile("global_load_dwordx4 %0, %2, %4 sc1\n\tglobal_load_dwordx4 %1, %3, %4 sc1" \
                                : "=&v"(fb[(k) % BWN][0]), "=&v"(fb[(k) % BWN][1]) : "v"(l16), "v"(l17), "s"(wp + (k) * 1024) : "memory")
#define A_ISSUE(k) asm volatile("global_load_dwordx4 %0, %2, %4 sc1\n\tglobal_load_dwordx4 %1, %3, %4 sc1" \
                                : "=&v"(fa[(k) % AWN][0]), "=&v"(fa[(k) % AWN][1]) : "v"(l16), "v"(l17), "s"(ha + (k) * 1024) : "memory")
#pragma unroll
      for (int k = 0; k < BWN; ++k) B_ISSUE(k);
      {
        const unsigned fo = (unsigned)WS_FH + (rb * 8 + (lane & 7)) * 128;
        u32x2 f;
#pragma unroll 1
        for (unsigned spins = 0; spins < (1u << 20); ++spins) {
          asm volatile("global_load_dwordx2 %0, %1, %2 sc1\n\ts_waitcnt vmcnt(0)" : "=&v"(f) : "v"(fo), "s"(ws) : "memory");
          if (!wave_any(f[0] != (unsigned)en || f[1] != (unsigned)(en >> 32))) break;
          __builtin_amdgcn_s_sleep(3);
        }
      }
#pragma unroll
      for (int k = 0; k < AWN; ++k) A_ISSUE(k);
      f32x16 g00, g01, g10, g11;
#pragma unroll
      for (int i = 0; i < 16; ++i) { g00[i] = 0.f; g01[i] = 0.f; g10[i] = 0.f; g11[i] = 0.f; }
#pragma unroll
      for (int k = 0; k < 16; ++k) {
        asm volatile("s_waitcnt vmcnt(%4)" : "+v"(fa[k % AWN][0]), "+v"(fa[k % AWN][1]), "+v"(fb[k % BWN][0]), "+v"(fb[k % BWN][1])
                     : "i"(g2_behind(k)) : "memory");
        const u32x4 a0 = fa[k % AWN][0], a1 = fa[k % AWN][1], b0 = fb[k % BWN][0], b1_ = fb[k % BWN][1];
        g00 = __builtin_amdgcn_mfma_f32_32x32x16_bf16(as_bf8(a0), as_bf8(b0), g00, 0, 0, 0);
        g01 = __builtin_amdgcn_mfma_f32_32x32x16_bf16(as_bf8(a0), as_bf8(b1_), g01, 0, 0, 0);
        g10 = __builtin_amdgcn_mfma_f32_32x32x16_bf16(as_bf8(a1), as_bf8(b0), g10, 0, 0, 0);
        g11 = __builtin_amdgcn_mfma_f32_32x32x16_bf16(as_bf8(a1), as_bf8(b1_), g11, 0, 0, 0);
        SCHED();
        if (k + BWN < 16) B_ISSUE(k + BWN);
        if (k + AWN < 16) A_ISSUE(k + AWN);
        if (k == 16 - AWN) LOAD_FINAL();
        if (k == S_GO) __hip_atomic_store(fl + 13, 1u, __ATOMIC_RELAXED, __HIP_MEMORY_SCOPE_WORKGROUP);
        SCHED();
      }
      WAIT_FINAL();
      lds_wait(fl + 11, 4u);
      char* red = smem + RED_OFF(w);
#pragma unroll
      for (int g = 0; g < 4; ++g) {
        *(f32x4*)(red + r * 256 + (((2 * g + h) ^ (r & 15)) << 4)) = (f32x4){g00[4 * g], g00[4 * g + 1], g00[4 * g + 2], g00[4 * g + 3]};
        *(f32x4*)(red + (32 + r) * 256 + (((2 * g + h) ^ (r & 15)) << 4)) = (f32x4){g01[4 * g], g01[4 * g + 1], g01[4 * g + 2], g01[4 * g + 3]};
        *(f32x4*)(red + r * 256 + (((8 + 2 * g + h) ^ (r & 15)) << 4)) = (f32x4){g10[4 * g], g10[4 * g + 1], g10[4 * g + 2], g10[4 * g + 3]};
        *(f32x4*)(red + (32 + r) * 256 + (((8 + 2 * g + h) ^ (r & 15)) << 4)) = (f32x4){g11[4 * g], g11[4 * g + 1], g11[4 * g + 2], g11[4 * g + 3]};
      }
      lds_arrive(lds0 + K1_FL + 48);
    }
  } else {
    const int sv = w - 7, chl = sv >> 1, hf = sv & 1, c = 2 * (xcd * 32 + jj) + chl, tb = rb * 8 + cb;
    if (sv == 3 && lane < 20) fl[lane] = 0u;
    BARRIER();
    lds_wait(fl, 7u);
    u32x4 xr[2];
    u32x2 ep;
    unsigned q_lar, q_aim, q_cre, q_cim, q_cr2, q_ci2, q_ldt, q_ds;
    {
      const unsigned vo = (unsigned)((tb * 8 + 2 * sv + h) * 1024 + r * 16), z = 0;
      const unsigned pvo = c * 256 + hf * 128 + lane * 2, pv2 = pvo ^ 128u, pvd = c * 2;
      asm volatile(
          "global_load_dwordx4 %0, %11, %16\n\tglobal_load_dwordx4 %1, %11, %16 offset:512\n\t"
          "global_load_dwordx2 %2, %12, %17 sc1\n\t"
          "global_load_ushort %3, %13, %18\n\tglobal_load_ushort %4, %13, %19\n\tglobal_load_ushort %5, %13, %20\n\t"
          "global_load_ushort %6, %13, %21\n\tglobal_load_ushort %7, %14, %20\n\tglobal_load_ushort %8, %14, %21\n\t"
          "global_load_ushort %9, %15, %22\n\tglobal_load_ushort %10, %15, %23"
          : "=&v"(xr[0]), "=&v"(xr[1]), "=&v"(ep), "=&v"(q_lar), "=&v"(q_aim), "=&v"(q_cre), "=&v"(q_cim), "=&v"(q_cr2),
            "=&v"(q_ci2), "=&v"(q_ldt), "=&v"(q_ds)
          : "v"(vo), "v"(z), "v"(pvo), "v"(pv2), "v"(pvd), "s"(xb), "s"(ws + WS_EP), "s"(lar), "s"(aim), "s"(cre), "s"(cim),
            "s"(ldt), "s"(dskip)
          : "memory");
    }
    u32x4 dmy = {0u, 0u, 0u, 0u};
    if (sv < 2) {
      unsigned long long pc;
      int len;
      asm volatile("s_getpc_b64 %0\n.Ls4d_anchor:\n\ts_mov_b32 %1, s4d_code_end-.Ls4d_anchor" : "=s"(pc), "=s"(len));
      const unsigned off = min((jj * 2 + sv) * 1024 + lane * 16, len - 16);
      asm volatile("global_load_dwordx4 %0, %1, %2" : "+v"(dmy) : "v"(off), "s"(pc) : "memory");
    }
    {
      const unsigned vo = (lane >> 2) * 1024 + (lane & 3) * 16;
      const int g0 = b * 7 + sv, g1 = b * 7 + min(sv + 4, 6);
      dma1(lds0 + K1_W2 + sv * 1024, (const char*)w2 + (g0 >> 4) * 16384 + (g0 & 15) * 64, vo);
      if (sv < 3) dma1(lds0 + K1_W2 + (sv + 4) * 1024, (const char*)w2 + (g1 >> 4) * 16384 + (g1 & 15) * 64, vo);
    }
    asm volatile("s_waitcnt vmcnt(0)" : "+v"(xr[0]), "+v"(xr[1]), "+v"(ep), "+v"(q_lar), "+v"(q_aim), "+v"(q_cre), "+v"(q_cim),
                 "+v"(q_cr2), "+v"(q_ci2), "+v"(q_ldt), "+v"(q_ds), "+v"(dmy)::"memory");
    ln_stats(xr, ep, ws, lane, tb * 8 + 2 * sv, smem + S_TILE + (7 - 2 * sv - h) * 1024 + r * 16);
    lds_arrive(lds0 + K1_FL + 64);
    const unsigned long long en = (((unsigned long long)ep[1] << 32) | ep[0]) + 1ull;
    {
      const char* p = smem + K1_W2 + sv * 1024 + (4 * h + q4) * 64 + (16 * blk + 4 * p4) * 2;
      const u32x2 a = tr_rd(p), cc = tr_rd(p + 512);
      const int g0 = b * 7 + sv, g1 = g0 + 4;
      st_wt(ws + WS_W2F + (size_t)((g0 & 15) * 112 + (g0 >> 4)) * 1024 + lane * 16, (u32x4){a[0], a[1], cc[0], cc[1]});
      if (sv < 3) {
        const u32x2 a2 = tr_rd(p + 4096), c2 = tr_rd(p + 4096 + 512);
        st_wt(ws + WS_W2F + (size_t)((g1 & 15) * 112 + (g1 >> 4)) * 1024 + lane * 16, (u32x4){a2[0], a2[1], c2[0], c2[1]});
      }
    }
    lds_wait(fl + 16, 4u);
#pragma unroll
    for (int i = 0; i < 2; ++i) {
      const char* p = smem + S_TILE + q4 * 1024 + (128 * sv + 64 * i + (lane >> 4) * 16 + p4 * 4) * 2;
      const u32x2 a = tr_rd(p), cc = tr_rd(p + 4096);
      st_wt(ws + WS_UT + (size_t)tb * 8192 + (128 * sv + 64 * i + lane) * 16, (u32x4){a[0], a[1], cc[0], cc[1]});
    }
    const float dt = rbf(expf(bflo(q_ldt)));
    const float mx = fmaxf(fmaxf(fabsf(bflo(q_cre)), fabsf(bflo(q_cr2))), fmaxf(fabsf(bflo(q_cim)), fabsf(bflo(q_ci2))));
    const int ce = max(__builtin_amdgcn_frexp_expf(wave_absmax(mx) * dt), -100);
    const float dsc = dt * __builtin_amdgcn_ldexpf(1.f, -ce), kun = __builtin_amdgcn_ldexpf(1.f, ce + 1);
    const float zr2 = -rbf(expf(bflo(q_lar))) * dt * 1.44269504f, zi = bflo(q_aim) * dt;
    const f32x2 e1 = cexp_p(zr2, -zi, 1.f), w8 = cexp_p(zr2, zi, 8.f), G = cexp_p(zr2, -zi, 256.f);
    const f32x2 cc0 = {bflo(q_cre) * dsc, -bflo(q_cim) * dsc};
    WAIT_VM(0);
    if (lane == 0)
      __hip_atomic_store((GAS unsigned long long*)(ws + WS_FU + (sv * 256 + tb) * 8), en, __ATOMIC_RELAXED, __HIP_MEMORY_SCOPE_AGENT);
    if (sv == 0) {
      const unsigned goff = (unsigned)WS_GR + (rb * 64 + lane) * 16;
      const unsigned tl = (unsigned)(en << 16), th = (unsigned)(en >> 16);
      u32x4 gr;
#pragma unroll 1
      for (unsigned spins = 0; spins < (1u << 20); ++spins) {
        asm volatile("global_load_dwordx4 %0, %1, %2 sc1\n\ts_waitcnt vmcnt(0)" : "=&v"(gr) : "v"(goff), "s"(ws) : "memory");
        const bool ok = (gr[0] & 0xffff0000u) == tl && gr[1] == th && (gr[2] & 0xffff0000u) == tl && gr[3] == th;
        if (!wave_any(!ok)) break;
        __builtin_amdgcn_s_sleep(2);
      }
      *(unsigned*)(smem + K1_MS + lane * 4) = (gr[0] & 0xffffu) | (gr[2] << 16);
      if (lane == 0) *(unsigned long long*)(smem + K1_KS + 16) = en;
      lds_set(fl + 1, 1u);
    }
    char* tsl = smem + chl * STG;
    lds_wait(fl, 7 * (6 + chl));
    __builtin_amdgcn_s_setprio(SVC_PRIO2);
    tab_gen(tsl + hf * TAB + lane * 4, cc0, e1, w8, G);
    f32x16 ka;
#pragma unroll
    for (int i = 0; i < 16; ++i) ka[i] = 0.f;
    {
      const char* tbr = tsl + hf * TAB + r * 272 + h * 16;
      u32x4 ta[8], tc[8];
#pragma unroll
      for (int k = 0; k < 8; ++k) { ta[k] = *(const u32x4*)(tbr + k * 32); tc[k] = *(const u32x4*)(tbr + 21 * 272 + k * 32); }
#pragma unroll
      for (int k = 0; k < 8; ++k) ka = __builtin_amdgcn_mfma_f32_32x32x16_f16(as_h8(ta[k]), as_h8(tc[k]), ka, 0, 0, 0);
      char* xk = tsl + 2 * TAB + hf * 3072 + lane * 16;
#pragma unroll
      for (int g = 0; g < 3; ++g) *(f32x4*)(xk + g * 1024) = (f32x4){ka[4 * g], ka[4 * g + 1], ka[4 * g + 2], ka[4 * g + 3]};
      lds_set(fl + 2 + sv, 1u);
    }
    u32x4 u0, u1;
    {
      const unsigned foff = (unsigned)WS_FU + (c >> 7) * 2048 + lane * 16;
      u32x4 f0, f1;
#pragma unroll 1
      for (unsigned spins = 0; spins < (1u << 20); ++spins) {
        asm volatile("global_load_dwordx4 %0, %2, %3 sc1\n\tglobal_load_dwordx4 %1, %2, %3 offset:1024 sc1\n\ts_waitcnt vmcnt(0)"
                     : "=&v"(f0), "=&v"(f1) : "v"(foff), "s"(ws) : "memory");
        const unsigned lo = (unsigned)en, hi = (unsigned)(en >> 32);
        if (!wave_any(f0[0] != lo || f0[1] != hi || f0[2] != lo || f0[3] != hi || f1[0] != lo || f1[1] != hi || f1[2] != lo || f1[3] != hi)) break;
        __builtin_amdgcn_s_sleep(2);
      }
      const unsigned uo = (unsigned)WS_UT + (255 - 128 * hf - lane) * 8192 + c * 16;
      asm volatile("global_load_dwordx4 %0, %2, %4 sc1\n\tglobal_load_dwordx4 %1, %3, %4 sc1"
                   : "=&v"(u0), "=&v"(u1) : "v"(uo), "v"(uo - 64 * 8192), "s"(ws) : "memory");
    }
    char* cv = smem + (2 + chl) * STG;
    lds_wait(fl + 2 + (sv ^ 1), 1u);
    unsigned D[12];
    {
      const char* xo = tsl + 2 * TAB + (hf ^ 1) * 3072 + lane * 16;
#pragma unroll
      for (int g = 0; g < 3; ++g) {
        const f32x4 o = *(const f32x4*)(xo + g * 1024);
#pragma unroll
        for (int e = 0; e < 4; ++e) { const float v = ka[4 * g + e] + o[e]; D[4 * g + e] = pk_f16(v, v); }
      }
    }
    if (chl) lds_wait(fl + 10, 7u); else lds_wait(fl, 7 * 8);
    {
      char* c0 = cv + CV_CP + 32;
      const bool last = (r == 31);
      const unsigned f512 = __builtin_amdgcn_readlane(D[4], 32), f768 = __builtin_amdgcn_readlane(D[6], 32),
                     f1024 = __builtin_amdgcn_readlane(D[8], 0), f1280 = __builtin_amdgcn_readlane(D[9], 0),
                     f1536 = __builtin_amdgcn_readlane(D[10], 0), f1792 = __builtin_amdgcn_readlane(D[11], 0),
                     f2048 = __builtin_amdgcn_readlane(D[8], 32);
      *(u32x2*)(c0 + (8 * r + 4 * h) * 2) = (u32x2){mix(D[0], D[1]), mix(D[2], D[3])};
      {
        const unsigned n4 = nxt(D[4]);
        const unsigned nn = last ? (h ? f768 : f512) : n4;
        const unsigned e1_ = h ? D[4] : D[5], e2_ = h ? D[5] : D[6], e3_ = h ? nn : D[7];
        *(u32x4*)(c0 + (256 + 256 * h + 8 * r) * 2) = (u32x4){D[4], mix(e1_, e2_), e2_, mix(e3_, nn)};
      }
      {
        const unsigned k0 = h ? D[6] : D[8];
        const unsigned n0 = nxt(k0);
        const unsigned nn = last ? (h ? f1024 : f1280) : n0;
        const unsigned e1_ = h ? D[7] : k0, e2_ = h ? D[7] : ((r & 1) ? nn : k0), e3_ = h ? D[7] : nn;
        *(u32x4*)(c0 + (h ? 768 + 8 * r : 1024 + 8 * r) * 2) = (u32x4){k0, mix(k0, e1_), mix(e2_, e3_), nn};
      }
      const unsigned fl[3] = {f1536, f1792, f2048};
#pragma unroll
      for (int a = 0; a < 3; ++a) {
        const unsigned k0 = D[9 + a], n0 = nxt(k0), nn = last ? fl[a] : n0;
        const u32x4 v = {k0, k0, (r & 1) ? nn : mix(k0, nn), nn};
        *(u32x4*)(h ? cv + CV_CP + (r & 1) * 16 : c0 + (1280 + 256 * a + 8 * r) * 2) = h ? (u32x4){0u, 0u, 0u, 0u} : v;
      }
    }
    if (lane < 16) *(u32x4*)(cv + CV_U + 4096 + hf * 256 + lane * 16) = (u32x4){0u, 0u, 0u, 0u};
    asm volatile("s_waitcnt vmcnt(0)" : "+v"(u0), "+v"(u1)::"memory");
    *(u32x4*)(cv + CV_U + hf * 2048 + lane * 16) = u0;
    *(u32x4*)(cv + CV_U + hf * 2048 + 1024 + lane * 16) = u1;
    lds_set(fl + 2 + sv, 2u);
    lds_wait(fl + 2 + (sv ^ 1), 2u);
    conv_run(cv, ws, fl, c, sv, lane, kun, bflo(q_ds), en);
    __builtin_amdgcn_s_setprio(0);
    if (sv == 1) {
      lds_wait(fl + 13, 1u);
      const unsigned fo = (unsigned)WS_FS + cb * 1024 + lane * 16;
      u32x4 f;
#pragma unroll 1
      for (unsigned spins = 0; spins < (1u << 20); ++spins) {
        asm volatile("global_load_dwordx4 %0, %1, %2 sc1\n\ts_waitcnt vmcnt(0)" : "=&v"(f) : "v"(fo), "s"(ws) : "memory");
        if (!wave_any(f[0] != (unsigned)en || f[1] != (unsigned)(en >> 32) || f[2] != (unsigned)en || f[3] != (unsigned)(en >> 32))) break;
        __builtin_amdgcn_s_sleep(4);
      }
      const char* sp = ws + WS_US + (size_t)(rb * 512 + cb * 64) * 128;
      const unsigned l16 = lane * 16, l17 = 4096 + lane * 16;
      u32x4 sv[8];
      asm volatile("global_load_dwordx4 %0, %8, %10 sc1\n\tglobal_load_dwordx4 %1, %8, %10 offset:1024 sc1\n\t"
                   "global_load_dwordx4 %2, %8, %10 offset:2048 sc1\n\tglobal_load_dwordx4 %3, %8, %10 offset:3072 sc1\n\t"
                   "global_load_dwordx4 %4, %9, %10 sc1\n\tglobal_load_dwordx4 %5, %9, %10 offset:1024 sc1\n\t"
                   "global_load_dwordx4 %6, %9, %10 offset:2048 sc1\n\tglobal_load_dwordx4 %7, %9, %10 offset:3072 sc1\n\ts_waitcnt vmcnt(0)"
                   : "=&v"(sv[0]), "=&v"(sv[1]), "=&v"(sv[2]), "=&v"(sv[3]), "=&v"(sv[4]), "=&v"(sv[5]), "=&v"(sv[6]), "=&v"(sv[7])
                   : "v"(l16), "v"(l17), "s"(sp) : "memory");
#pragma unroll
      for (int i = 0; i < 8; ++i)
        *(u32x4*)(smem + S_TILE + (8 * i + (lane >> 3)) * 128 + (((lane & 7) ^ (lane >> 3)) << 4)) = sv[i];
      lds_arrive(lds0 + K1_FL + 48);
    }
    if (sv) {
      return;
    }
    LOAD_FINAL();
    WAIT_FINAL();
  }
  while ((unsigned)__builtin_amdgcn_readfirstlane(__hip_atomic_load(fl + 12, __ATOMIC_RELAXED, __HIP_MEMORY_SCOPE_WORKGROUP)) < 8u)
    __builtin_amdgcn_s_sleep(1);
  asm volatile("" ::: "memory");
  {
    f32x4 v0 = {0.f, 0.f, 0.f, 0.f}, v1 = {0.f, 0.f, 0.f, 0.f};
#pragma unroll
    for (int q = 0; q < 7; ++q) {
      v0 += *(const f32x4*)(smem + RED_OFF(q) + lane * 256 + (((2 * fw) ^ (lane & 15)) << 4));
      v1 += *(const f32x4*)(smem + RED_OFF(q) + lane * 256 + (((2 * fw + 1) ^ (lane & 15)) << 4));
    }
    const u32x4 sv = *(const u32x4*)(smem + S_TILE + lane * 128 + (((7 - fw) ^ (lane & 7)) << 4));
    const float bb = bflo(b2v);
    u16* op = out + (size_t)(rb * 64 + fw * 8) * D_ + cb * 64 + lane;
#pragma unroll
    for (int j = 0; j < 4; ++j) {
      const float a0 = j < 2 ? v0[2 * j] : v1[2 * j - 4], a1 = j < 2 ? v0[2 * j + 1] : v1[2 * j - 3];
      const unsigned pa = pk_bf16(a0, a1);
      const unsigned pv = pk_bf16(bflo(pa) + bb, bfhi(pa) + bb);
      const float q0 = bflo(pv), q1 = bfhi(pv);
      const unsigned pg = pk_bf16(q0 * __builtin_amdgcn_rcpf(1.f + __builtin_amdgcn_exp2f(q0 * -1.44269504f)),
                                  q1 * __builtin_amdgcn_rcpf(1.f + __builtin_amdgcn_exp2f(q1 * -1.44269504f)));
      const unsigned ps = pk_bf16(bfhi(sv[3 - j]) * bflo(pg), bflo(sv[3 - j]) * bfhi(pg));
      const unsigned po = pk_bf16(bflo(xv[2 * j]) + bflo(ps), bflo(xv[2 * j + 1]) + bfhi(ps));
      op[(size_t)(2 * j) * D_] = (u16)po;
      op[(size_t)(2 * j + 1) * D_] = (u16)(po >> 16);
    }
  }
  asm volatile("s4d_code_end:");
}

extern "C" void kernel_launch(void* const* d_in, const int* in_sizes, int n_in, void* d_out, int out_size, void* d_ws,
                              size_t ws_size, hipStream_t stream) {
  (void)in_sizes; (void)n_in; (void)out_size; (void)ws_size;
  (void)hipFuncSetAttribute((const void*)k1_ffn_up, hipFuncAttributeMaxDynamicSharedMemorySize, K1_LDS);
  k1_ffn_up<<<256, 704, K1_LDS, stream>>>((const u16*)d_in[0], (const u16*)d_in[8], (const u16*)d_in[9], (const u16*)d_in[10],
                                          (const u16*)d_in[1], (const u16*)d_in[2], (const u16*)d_in[3], (const u16*)d_in[4],
                                          (const u16*)d_in[5], (const u16*)d_in[6], (const u16*)d_in[11], (char*)d_ws,
                                          (u16*)d_out);
}
```
